```python
import torch

def _softmax(x, axis=-1):
    return torch.softmax(x, dim=axis)
def _gelu(x):
    return torch.nn.functional.gelu(x, approximate="tanh")
def _silu(x):
    return torch.nn.functional.silu(x)
def _sigmoid(x):
    return torch.sigmoid(x)
def _layernorm(x, axis=-1, eps=1e-5):
    m = x.mean(dim=axis, keepdim=True)
    v = x.var(dim=axis, keepdim=True, unbiased=False)
    return (x - m) / torch.sqrt(v + eps)
def _scatter_add(tgt, idx, src, axis=0):
    return tgt.clone().index_add_(axis, idx, src)
def _segment_sum(src, seg, n):
    out = torch.zeros((n,) + src.shape[1:], dtype=src.dtype)
    return out.index_add_(0, seg, src)
def _pad(x, pw):
    flat = tuple(v for pair in reversed(pw) for v in pair)
    return torch.nn.functional.pad(x, flat)

kernel_name = 'parallel_sum__sliding_block__glu_gated_geglu__prompt'

def setup_inputs():

    _w = (lambda a: (lambda t: t.to(torch.bfloat16) if t.is_floating_point() else t)(torch.as_tensor(a)))
    def _A_setup():
        B, S, D = 1, 2048, 512
        return {
            "x": _w((lambda *s: torch.randn(*s, dtype=torch.bfloat16) * 0.05)(B, S, D)),
            "wqkv": _w((lambda *s: torch.randn(*s, dtype=torch.bfloat16) * 0.05)(D, 3 * D)),
            "wo": _w((lambda *s: torch.randn(*s, dtype=torch.bfloat16) * 0.05)(D, D)),
        }
    _A_in = _A_setup()

    def _B_setup():
        B, S, Dm, Df = 1, 2048, 512, 1792
        return {
            "x": _w((lambda *s: torch.randn(*s, dtype=torch.bfloat16) * 0.05)(B, S, Dm)),
            "w_gate": _w((lambda *s: torch.randn(*s, dtype=torch.bfloat16) * 0.8)(Dm, Df) * (Dm ** -0.5)),
            "w_up": _w((lambda *s: torch.randn(*s, dtype=torch.bfloat16) * 0.8)(Dm, Df) * (Dm ** -0.5)),
            "w_down": _w((lambda *s: torch.randn(*s, dtype=torch.bfloat16) * 0.8)(Df, Dm) * (Df ** -0.5)),
        }
    _B_in = _B_setup()

    return {
        "A_x": _A_in["x"],
        "A_wqkv": _A_in["wqkv"],
        "A_wo": _A_in["wo"],
        "B_x": _B_in["x"],
        "B_w_gate": _B_in["w_gate"],
        "B_w_up": _B_in["w_up"],
        "B_w_down": _B_in["w_down"],
    }

def reference(A_x, A_wqkv, A_wo, B_x, B_w_gate, B_w_up, B_w_down):
    def _A_ref(x, wqkv, wo):
        B, S, D = x.shape
        H = 32; Dh = D // H; W = 256
        qkv = (lambda x, *d: x.permute(*d))((x @ wqkv).reshape(B, S, 3, H, Dh), 2, 0, 3, 1, 4)
        q, k, v = qkv[0], qkv[1], qkv[2]
        scores = torch.einsum("bhqd,bhkd->bhqk", q, k) * (Dh ** -0.5)
        idx = torch.arange(S)
        mask = (idx[:, None] - idx[None, :] >= 0) & (idx[:, None] - idx[None, :] < W)
        scores = torch.where(mask[None, None], scores, -1e9)
        p = _softmax(scores, axis=-1)
        o = torch.einsum("bhqk,bhkd->bhqd", p, v)
        return (lambda x, *d: x.permute(*d))(o, 0, 2, 1, 3).reshape(B, S, D) @ wo

    def _B_ref(x, w_gate, w_up, w_down):
        g = torch.einsum("bsd,df->bsf", x, w_gate)
        u = torch.einsum("bsd,df->bsf", x, w_up)
        h = _gelu(g) * u
        return torch.einsum("bsf,fd->bsd", h, w_down)

    x = A_x
    xn = _layernorm(x, axis=-1)
    return x + _A_ref(xn, A_wqkv, A_wo) + _B_ref(xn, B_w_gate, B_w_up, B_w_down)
```

```cpp
#include <hip/hip_runtime.h>
#include <hip/hip_bf16.h>
#include <stdint.h>

typedef unsigned short u16;
__device__ __forceinline__ float bf2f(u16 v){ return __uint_as_float(((unsigned)v)<<16); }
__device__ __forceinline__ u16 f2bf(float f){ unsigned u=__float_as_uint(f); u += 0x7fffu + ((u>>16)&1u); return (u16)(u>>16); }

constexpr int S=2048, D=512, F=1792, H=32, DH=16, W=256;

__global__ void k_ln(const u16* x, u16* xn){
  int r=blockIdx.x; int t=threadIdx.x;
  float v[8]; float s=0;
  for(int i=0;i<8;i++){ v[i]=bf2f(x[r*D+t*8+i]); s+=v[i]; }
  for(int o=32;o>=1;o>>=1) s+=__shfl_xor(s,o);
  float m=s/D; float q=0;
  for(int i=0;i<8;i++){ float d=v[i]-m; q+=d*d; }
  for(int o=32;o>=1;o>>=1) q+=__shfl_xor(q,o);
  float rs=rsqrtf(q/D+1e-5f);
  for(int i=0;i<8;i++) xn[r*D+t*8+i]=f2bf((v[i]-m)*rs);
}
__global__ void k_gemm(const u16* A, const u16* B, float* C, int M,int N,int K){
  __shared__ float As[16][17], Bs[16][17];
  int tx=threadIdx.x, ty=threadIdx.y; int row=blockIdx.y*16+ty, col=blockIdx.x*16+tx;
  float acc=0;
  for(int k0=0;k0<K;k0+=16){
    As[ty][tx]=bf2f(A[row*K+k0+tx]); Bs[ty][tx]=bf2f(B[(k0+ty)*N+col]);
    __syncthreads();
    for(int k=0;k<16;k++) acc+=As[ty][k]*Bs[k][tx];
    __syncthreads();
  }
  C[row*N+col]=acc;
}
__global__ void k_attn(const float* qkv, u16* o){
  int idx=blockIdx.x*blockDim.x+threadIdx.x; int q=idx/H, h=idx%H;
  float qv[DH]; for(int d=0;d<DH;d++) qv[d]=bf2f(f2bf(qkv[q*1536+h*DH+d]));
  int k0=q-W+1; if(k0<0)k0=0;
  float m=-1e30f;
  for(int k=k0;k<=q;k++){ float s=0; for(int d=0;d<DH;d++) s+=qv[d]*bf2f(f2bf(qkv[k*1536+512+h*DH+d])); s*=0.25f; m=fmaxf(m,s);}
  float l=0; float acc[DH]; for(int d=0;d<DH;d++)acc[d]=0;
  for(int k=k0;k<=q;k++){ float s=0; for(int d=0;d<DH;d++) s+=qv[d]*bf2f(f2bf(qkv[k*1536+512+h*DH+d])); s*=0.25f; float p=__expf(s-m); l+=p; for(int d=0;d<DH;d++) acc[d]+=p*bf2f(f2bf(qkv[k*1536+1024+h*DH+d])); }
  for(int d=0;d<DH;d++) o[q*D+h*DH+d]=f2bf(acc[d]/l);
}
__global__ void k_geglu(const float* g,const float* u,u16* h,int n){
  int i=blockIdx.x*blockDim.x+threadIdx.x; if(i>=n)return;
  float x=g[i]; float t=tanhf(0.7978845608028654f*(x+0.044715f*x*x*x));
  h[i]=f2bf(0.5f*x*(1.f+t)*u[i]);
}
__global__ void k_final(const u16* x,const float* a,const float* b,u16* out,int n){
  int i=blockIdx.x*blockDim.x+threadIdx.x; if(i>=n)return;
  out[i]=f2bf(bf2f(x[i])+a[i]+b[i]);
}

extern "C" void kernel_launch(void* const* d_in, const int* in_sizes, int n_in,
                              void* d_out, int out_size, void* d_ws, size_t ws_size,
                              hipStream_t stream) {
  const u16* x=(const u16*)d_in[0]; const u16* wqkv=(const u16*)d_in[1]; const u16* wo=(const u16*)d_in[2];
  const u16* wg=(const u16*)d_in[4]; const u16* wu=(const u16*)d_in[5]; const u16* wd=(const u16*)d_in[6];
  char* ws=(char*)d_ws;
  u16* xn=(u16*)ws; ws+=S*D*2;
  float* qkv=(float*)ws; ws+=(size_t)S*1536*4;
  float* g=(float*)ws; ws+=(size_t)S*F*4;
  float* u=(float*)ws; ws+=(size_t)S*F*4;
  u16* o=(u16*)ws; ws+=S*D*2;
  u16* h=(u16*)ws; ws+=(size_t)S*F*2;
  float* a=(float*)ws; ws+=(size_t)S*D*4;
  float* b=(float*)ws; ws+=(size_t)S*D*4;
  k_ln<<<S,64,0,stream>>>(x,xn);
  dim3 blk(16,16);
  k_gemm<<<dim3(1536/16,S/16),blk,0,stream>>>(xn,wqkv,qkv,S,1536,D);
  k_gemm<<<dim3(F/16,S/16),blk,0,stream>>>(xn,wg,g,S,F,D);
  k_gemm<<<dim3(F/16,S/16),blk,0,stream>>>(xn,wu,u,S,F,D);
  k_attn<<<S*H/256,256,0,stream>>>(qkv,o);
  k_geglu<<<(S*F+255)/256,256,0,stream>>>(g,u,h,S*F);
  k_gemm<<<dim3(D/16,S/16),blk,0,stream>>>(o,wo,a,S,D,D);
  k_gemm<<<dim3(D/16,S/16),blk,0,stream>>>(h,wd,b,S,D,F);
  k_final<<<(S*D+255)/256,256,0,stream>>>(x,a,b,(u16*)d_out,S*D);
}
```

```cpp
#include <hip/hip_runtime.h>
#include <hip/hip_bf16.h>
#include <stdint.h>
#include <type_traits>

typedef unsigned short u16;
typedef unsigned long long u64;
typedef __attribute__((ext_vector_type(8))) __bf16 bf16x8;
typedef __attribute__((ext_vector_type(2))) __bf16 bf16x2;
typedef __attribute__((ext_vector_type(4))) short s16x4;
typedef __attribute__((ext_vector_type(8))) short s16x8;
typedef __attribute__((ext_vector_type(16))) float f32x16;
typedef __attribute__((ext_vector_type(4))) float f32x4;
typedef __attribute__((ext_vector_type(4))) unsigned u32x4;
#define LAS __attribute__((address_space(3)))
#define GAS __attribute__((address_space(1)))
#define DI __device__ __forceinline__
typedef LAS char* lds_ptr;

#define MFMA(a, b, c) __builtin_amdgcn_mfma_f32_32x32x16_bf16((a), (b), (c), 0, 0, 0)
#define RLX_AGENT __ATOMIC_RELAXED, __HIP_MEMORY_SCOPE_AGENT

#define ROT1 ((ib + 2 * (rr & 3)) & 7)
#define ROT2 1
#define NODOT 0
constexpr int PX = 1024;
constexpr int PQ = 3072;
constexpr int PF = 3584;
constexpr size_t WS_A2 = 0;
constexpr size_t WS_KV = 9437184;
constexpr size_t WS_FL = WS_KV + 4194304;
constexpr size_t WS_ST = WS_FL + 131072;

template <int I, int N, class F>
DI void static_for(F&& f) {
  if constexpr (I < N) { f(std::integral_constant<int, I>{}); static_for<I + 1, N>(f); }
}

DI void dma16(unsigned lds_dst, unsigned voff, const void* sbase) {
  asm volatile("s_mov_b32 m0, %0\n\ts_nop 0\n\tglobal_load_lds_dwordx4 %1, %2" ::"s"(lds_dst), "v"(voff), "s"(sbase) : "memory");
}
template <int N>
DI void wait_bar() { asm volatile("s_waitcnt vmcnt(%0) lgkmcnt(0)\n\ts_barrier" ::"n"(N) : "memory"); }
DI void lds_bar() { asm volatile("s_waitcnt lgkmcnt(0)\n\ts_barrier" ::: "memory"); }

DI void gload4(u32x4 (&a)[4], unsigned voff, const void* sbase) {
  asm volatile("global_load_dwordx4 %0, %4, %5\n\tglobal_load_dwordx4 %1, %4, %5 offset:1024\n\t"
               "global_load_dwordx4 %2, %4, %5 offset:2048\n\tglobal_load_dwordx4 %3, %4, %5 offset:3072"
               : "=&v"(a[0]), "=&v"(a[1]), "=&v"(a[2]), "=&v"(a[3]) : "v"(voff), "s"(sbase) : "memory");
}
DI void gload1(u32x4& a, unsigned voff, const void* sbase) {
  asm volatile("global_load_dwordx4 %0, %1, %2" : "=&v"(a) : "v"(voff), "s"(sbase) : "memory");
}
DI void pin4(u32x4 (&a)[4]) { asm volatile("" : "+v"(a[0]), "+v"(a[1]), "+v"(a[2]), "+v"(a[3])); }
#define SBAR() __builtin_amdgcn_sched_barrier(0)
DI void pinf(float& x) { asm volatile("" : "+v"(x)); }
DI void pinu(unsigned& x) { asm volatile("" : "+v"(x)); }
DI void pinv(bf16x8& x) { asm volatile("" : "+v"(x)); }
template <int N>
DI void adv(unsigned (&a)[N], int d) {
#pragma unroll
  for (int j = 0; j < N; ++j) { a[j] += d; pinu(a[j]); }
}
DI bf16x8 tr_frag(lds_ptr p, int off1) {
  s16x4 a = __builtin_amdgcn_ds_read_tr16_b64_v4i16((LAS s16x4*)p);
  s16x4 b = __builtin_amdgcn_ds_read_tr16_b64_v4i16((LAS s16x4*)(p + off1));
  return __builtin_bit_cast(bf16x8, __builtin_shufflevector(a, b, 0, 1, 2, 3, 4, 5, 6, 7));
}
DI unsigned pk2(float a, float b) { bf16x2 v = {(__bf16)a, (__bf16)b}; return __builtin_bit_cast(unsigned, v); }
DI float dot2(unsigned a, unsigned b, float c) {
  return __builtin_amdgcn_fdot2_f32_bf16(__builtin_bit_cast(bf16x2, a), __builtin_bit_cast(bf16x2, b), c, false);
}
DI float sum8(bf16x8 f, float c) {
  u32x4 u = __builtin_bit_cast(u32x4, f);
#pragma unroll
  for (int j = 0; j < 4; ++j) c = dot2(u[j], 0x3f803f80u, c);
  return c;
}
DI float sq8(bf16x8 f, float c) {
  u32x4 u = __builtin_bit_cast(u32x4, f);
#pragma unroll
  for (int j = 0; j < 4; ++j) c = dot2(u[j], u[j], c);
  return c;
}
DI float swap32(float v) {
  auto r = __builtin_amdgcn_permlane32_swap(__float_as_uint(v), __float_as_uint(v), false, false);
  return (threadIdx.x & 32) ? __uint_as_float(r[0]) : __uint_as_float(r[1]);
}
DI void split3(float c, unsigned& t1, unsigned& t2, unsigned& t3) {
  t1 = __float_as_uint(c) & 0xffff0000u;
  float r = c - __uint_as_float(t1);
  t2 = __float_as_uint(r) & 0xffff0000u;
  float r2 = r - __uint_as_float(t2);
  t3 = __float_as_uint(r2) & 0xffff0000u;
}
DI unsigned hi2(unsigned lo, unsigned hi) { return (lo >> 16) | hi; }
DI bf16x8 corr_c(float c) {
  unsigned a, b, d; split3(c, a, b, d);
  u32x4 u = {hi2(a, a), hi2(b, b), hi2(a, d), hi2(b, d)};
  return __builtin_bit_cast(bf16x8, u);
}
DI bf16x8 corr_m(float m) {
  unsigned a, b, d; split3(m, a, b, d);
  u32x4 u = {hi2(a, b), hi2(a, b), hi2(d, a), hi2(d, b)};
  return __builtin_bit_cast(bf16x8, u);
}
DI float geglu(float g, float u) {
  const float c1 = -2.0f * 1.4426950408889634f * 0.7978845608028654f, c2 = c1 * 0.044715f;
  float e = __builtin_amdgcn_exp2f(g * __builtin_fmaf(g * g, c2, c1));
  return g * u * __builtin_amdgcn_rcpf(1.0f + e);
}
DI void store_sc1(__amdgpu_buffer_rsrc_t rs, unsigned voff, unsigned soff, u32x4 v) {
  __builtin_amdgcn_raw_buffer_store_b128(v, rs, voff + soff, 0, 16);
}
DI u32x4 h_frag(const f32x16& a, float rstd) {
  float hv[8];
#pragma unroll
  for (int r = 0; r < 8; ++r) hv[r] = geglu(a[r] * rstd, a[r + 8] * rstd);
  u32x4 o = {pk2(hv[0], hv[1]), pk2(hv[2], hv[3]), pk2(hv[4], hv[5]), pk2(hv[6], hv[7])};
  return o;
}
DI u32x4 sc_frag(const f32x16& a, int X, float s) {
  u32x4 o = {pk2(a[8 * X] * s, a[8 * X + 1] * s), pk2(a[8 * X + 2] * s, a[8 * X + 3] * s),
             pk2(a[8 * X + 4] * s, a[8 * X + 5] * s), pk2(a[8 * X + 6] * s, a[8 * X + 7] * s)};
  return o;
}

#define STAMP(k)
#define STAMP_DECL
#define STAMP_OUT(kid)

__global__ __launch_bounds__(512, 2) void k1(const char* __restrict__ x, const char* __restrict__ wqkv,
                                             const char* __restrict__ wg, const char* __restrict__ wu, char* ws) {
  __shared__ __attribute__((aligned(1024))) char smem[163840];
  STAMP_DECL
  STAMP(0);
  const lds_ptr L = (lds_ptr)smem;
  const unsigned L0 = (unsigned)(size_t)L;
  const int tid = threadIdx.x, lane = tid & 63;
  const int w = __builtin_amdgcn_readfirstlane(tid >> 6);
  const int m = w >> 1, n = w & 1;
  const int b = blockIdx.x, xcd = b & 7, rr = b >> 3;
  const int ib = (xcd >> 2) * 8 + (rr >> 2);
  const int idx = 2 * (xcd & 3) + (rr & 1);
  const bool typeA = (rr & 2) == 0;
  const int h = lane >> 5, r32 = lane & 31;
  const int blk = (lane >> 4) & 1, tq = (lane & 15) >> 2, tp = lane & 3;
  const int perm = m ^ n;
  const int rot = ROT1;
  auto kc = [&](int c) { return (c + rot) & 7; };

  const __amdgpu_buffer_rsrc_t rs_ws = __builtin_amdgcn_make_buffer_rsrc(ws, 0, (int)WS_ST, 0x00020000);

  const unsigned vo_x = (lane >> 3) * PX + (((lane & 7) ^ ((4 * (w & 1) + (lane >> 4)) & 7)) * 16);
  const unsigned sw_w = (lane & 7) ^ (4 * ((lane >> 4) & 1));
  const unsigned vo_g = (lane >> 3) * PF + sw_w * 16;
  const unsigned vo_u = (lane >> 3) * PF + (sw_w ^ 2) * 16;
  const char* xb = x + (size_t)(ib * 128 + 8 * w) * PX;

  unsigned ax[4], ag[2][4];
#pragma unroll
  for (int s = 0; s < 4; ++s) {
    const int sa = s ^ perm;
    ax[s] = r32 * 128 + (((2 * sa + h) ^ ((r32 >> 1) & 7)) * 16);
#pragma unroll
    for (int t = 0; t < 2; ++t)
      ag[t][s] = (16 * sa + 8 * h + tq) * 128 + (((2 * (2 * n + t) + (tp >> 1)) ^ (4 * ((tq >> 1) & 1)) ^ (blk * 2)) * 16) + (tp & 1) * 8 + blk * 16384;
  }

  bf16x8 xf[32];
  float s1 = 0.f, s2 = 0.f, rstd;
  bf16x8 mB;
  const f32x16 zero16 = {0, 0, 0, 0, 0, 0, 0, 0, 0, 0, 0, 0, 0, 0, 0, 0};

  if (!typeA) {
    constexpr int SLOT = 32768, SCR = 131072;
    const char* gb = wg + (size_t)(8 * w) * PF + idx * 3 * 128;
    const char* ub = wu + (size_t)(8 * w) * PF + idx * 3 * 128;
    auto issue1 = [&](auto S_, auto J_) {
      constexpr int s = decltype(S_)::value, j = decltype(J_)::value;
      const unsigned dst = L0 + (s & 3) * SLOT + w * 1024;
      if constexpr (s < 8) {
        const int c = kc(s);
        if constexpr (j == 0) dma16(dst + 8192, vo_x, xb + c * 128);
        if constexpr (j == 1) dma16(dst + 24576, vo_x, xb + c * 128 + 64 * PX);
        if constexpr (j == 2) dma16(dst, vo_g, gb + c * 64 * PF);
        if constexpr (j == 3) dma16(dst + 16384, vo_u, ub + c * 64 * PF);
      } else if constexpr (s < 16) {
        constexpr int pp = (s - 8) / 4 + 1;
        const int c0 = kc(2 * ((s - 8) % 4)), c1 = kc(2 * ((s - 8) % 4) + 1);
        if constexpr (j == 0) dma16(dst, vo_g, gb + c0 * 64 * PF + pp * 128);
        if constexpr (j == 1) dma16(dst + 16384, vo_u, ub + c0 * 64 * PF + pp * 128);
        if constexpr (j == 2) dma16(dst + 8192, vo_g, gb + c1 * 64 * PF + pp * 128);
        if constexpr (j == 3) dma16(dst + 24576, vo_u, ub + c1 * 64 * PF + pp * 128);
      }
    };
    auto issue = [&](auto S_) { static_for<0, 4>([&](auto J_) { issue1(S_, J_); }); };
    const unsigned xoff = 8192 + (m & 1) * 4096 + (m >> 1) * 16384;
    bf16x8 wf[2][2];
    f32x16 acc[2] = {zero16, zero16};
    float cs[2] = {0.f, 0.f};
    auto rd = [&](auto G_) {
      constexpr int g = decltype(G_)::value;
      constexpr int st_ = g < 32 ? g / 4 : 8 + (g - 32) / 8;
      constexpr int sub = g < 32 ? 0 : ((g - 32) % 8) / 4;
      constexpr int base = (st_ & 1) * SLOT + sub * 8192;
      wf[g & 1][0] = tr_frag(L + base + ag[0][g & 3], 512);
      wf[g & 1][1] = tr_frag(L + base + ag[1][g & 3], 512);
      if constexpr (g < 32) xf[g] = *(LAS bf16x8*)(L + (st_ & 1) * SLOT + ax[g & 3]);
    };
    adv(ax, xoff);
    static_for<0, 4>(issue);
    wait_bar<12>();
    STAMP(1);
    rd(std::integral_constant<int, 0>{});
    static_for<0, 96>([&](auto G_) {
      constexpr int g = decltype(G_)::value;
      constexpr int st_ = g < 32 ? g / 4 : 8 + (g - 32) / 8;
      constexpr bool last = g < 32 ? (g % 4 == 3) : ((g - 32) % 8 == 7);
      if constexpr (last && st_ + 1 < 16) {
        constexpr int hi = st_ + 3 < 15 ? st_ + 3 : 15;
        wait_bar<4 * (hi - (st_ + 1))>();
        if constexpr (st_ & 1) {
          constexpr int d = (st_ & 2) ? -65536 : 65536;
          adv(ag[0], d); adv(ag[1], d);
          if constexpr (st_ < 7) adv(ax, d);
        }
      }
      if constexpr (g + 1 < 96) rd(std::integral_constant<int, g + 1>{});
      acc[0] = MFMA(wf[g & 1][0], xf[g & 31], acc[0]);
      {
        constexpr int pos = g < 32 ? g % 4 : (g - 32) % 8;
        if constexpr (last) issue1(std::integral_constant<int, st_ + 4>{}, std::integral_constant<int, 0>{});
        else if constexpr (st_ >= 1 && g < 32) issue1(std::integral_constant<int, st_ + 3>{}, std::integral_constant<int, pos + 1>{});
        else if constexpr (g >= 32 && (pos & 1)) issue1(std::integral_constant<int, st_ + 3>{}, std::integral_constant<int, (pos + 1) / 2>{});
      }
      acc[1] = MFMA(wf[g & 1][1], xf[g & 31], acc[1]);
      if constexpr ((g & 3) == 0 && !NODOT) { cs[0] = sum8(wf[g & 1][0], cs[0]); cs[1] = sum8(wf[g & 1][1], cs[1]); pinf(cs[0]); pinf(cs[1]); }
      if constexpr (g < 32 && (g & 1) == 0 && !NODOT) { s1 = sum8(xf[g], s1); s2 = sq8(xf[g], s2); pinf(s1); pinf(s2); }
      SBAR();
      if constexpr ((g & 31) == 31) {
        constexpr int pp = g / 32;
        STAMP(2 + 2 * pp);
        LAS float* scs = (LAS float*)(L + SCR + (pp & 1) * 4096);
        scs[((2 * n + 0) * 4 + m) * 64 + lane] = cs[0];
        scs[((2 * n + 1) * 4 + m) * 64 + lane] = cs[1];
        LAS float* sst = (LAS float*)(L + SCR + 8192);
        if constexpr (pp == 0) {
          const float a1 = s1 + swap32(s1), a2 = s2 + swap32(s2);
          sst[(m * 2 + n) * 64 + lane] = h ? a2 : a1;
        }
        lds_bar();
        if constexpr (pp == 0) {
          const float t1 = sst[(m * 2) * 64 + r32] + sst[(m * 2 + 1) * 64 + r32];
          const float t2 = sst[(m * 2) * 64 + 32 + r32] + sst[(m * 2 + 1) * 64 + 32 + r32];
          const float mean = t1 * (1.f / 512.f);
          const float var = __builtin_fmaxf(t2 * (1.f / 512.f) - mean * mean, 0.f);
          rstd = __builtin_amdgcn_rsqf(var + 1e-5f);
          mB = corr_m(-mean);
        }
#pragma unroll
        for (int t = 0; t < 2; ++t) {
          const LAS float* p = scs + (2 * n + t) * 256 + lane;
          const float c = (p[0] + p[64]) + (p[128] + p[192]);
          acc[t] = MFMA(corr_c(c), mB, acc[t]);
        }
#pragma unroll
        for (int t = 0; t < 2; ++t) {
          const int ks = 4 * (idx * 3 + pp) + 2 * n + t;
          store_sc1(rs_ws, lane * 16, (unsigned)(WS_A2 + ((size_t)(ib * 4 + m) * 144 + ks) * 1024), h_frag(acc[t], rstd));
          acc[t] = zero16; cs[t] = 0.f;
        }
        STAMP(3 + 2 * pp);
      }
    });
  } else {
    constexpr int SLOT = 40960, OWN = 122880, KVB = OWN - 65536, SCR = 155648;
    GAS u64* fl = (GAS u64*)(ws + WS_FL);
    const int cb = ib + (lane < 3 ? lane : 0);
    const u64 ep = __hip_atomic_load(fl + (size_t)((cb < 16 ? cb : ib) * 8 + idx) * 64, RLX_AGENT);
    const int hf = idx & 1, pairX = 24 + (idx >> 1);
    const bool act = (w >> 2) == hf;
    const unsigned vo_q = (lane >> 3) * PQ + sw_w * 16;
    const char* qb = wqkv + (size_t)(8 * w) * PQ + idx * 128;
    const char* gb = wg + (size_t)(8 * w) * PF + pairX * 128;
    const char* ub = wu + (size_t)(8 * w) * PF + pairX * 128;
    auto issue1 = [&](auto S_, auto J_) {
      constexpr int s = decltype(S_)::value, j = decltype(J_)::value;
      const unsigned dst = L0 + (s % 3) * SLOT + w * 1024;
      if constexpr (s < 8) {
        const int c = kc(s);
        if constexpr (j == 0) dma16(dst, vo_x, xb + c * 128);
        if constexpr (j == 1) dma16(dst + 8192, vo_x, xb + c * 128 + 64 * PX);
        if constexpr (j == 2) dma16(dst + 16384, vo_q, qb + c * 64 * PQ);
        if constexpr (j == 3) dma16(dst + 24576, vo_q, qb + c * 64 * PQ + 1024);
        if constexpr (j == 4) dma16(dst + 32768, vo_q, qb + c * 64 * PQ + 2048);
      } else {
        const int c0 = kc(2 * (s - 8)), c1 = kc(2 * (s - 8) + 1);
        if constexpr (j == 0) dma16(dst, vo_g, gb + c0 * 64 * PF);
        if constexpr (j == 1) dma16(dst + 16384, vo_u, ub + c0 * 64 * PF);
        if constexpr (j == 2) dma16(dst + 8192, vo_g, gb + c1 * 64 * PF);
        if constexpr (j == 3) dma16(dst + 24576, vo_u, ub + c1 * 64 * PF);
      }
    };
    auto issue = [&](auto S_) { static_for<0, 5>([&](auto J_) { issue1(S_, J_); }); };
    unsigned aq[4];
#pragma unroll
    for (int s = 0; s < 4; ++s)
      aq[s] = (16 * (s ^ perm) + 8 * h + tq) * 128 + (((4 * n + 2 * blk + (tp >> 1)) ^ (4 * ((tq >> 1) & 1))) * 16) + (tp & 1) * 8;
    bf16x8 wf[2][3];
    f32x16 acc[3] = {zero16, zero16, zero16};
    float cs[3] = {0.f, 0.f, 0.f};
    auto rd = [&](auto G_) {
      constexpr int g = decltype(G_)::value;
#pragma unroll
      for (int t = 0; t < 3; ++t) wf[g & 1][t] = tr_frag(L + 16384 + t * 8192 + aq[g & 3], 512);
      xf[g] = *(LAS bf16x8*)(L + ax[g & 3]);
    };
    adv(ax, m * 4096);
    static_for<0, 3>(issue);
    wait_bar<10>();
    STAMP(1);
    rd(std::integral_constant<int, 0>{});
    static_for<0, 32>([&](auto G_) {
      constexpr int g = decltype(G_)::value;
      constexpr int st_ = g / 4;
      if constexpr (g % 4 == 3) {
        wait_bar<(st_ + 2 < 8 ? 5 : 4)>();
        if constexpr (st_ < 7) {
          constexpr int d = (st_ % 3 == 2) ? -2 * SLOT : SLOT;
          adv(aq, d); adv(ax, d);
        }
      }
      if constexpr (g + 1 < 32) rd(std::integral_constant<int, g + 1>{});
      acc[0] = MFMA(wf[g & 1][0], xf[g], acc[0]);
      if constexpr (g == 31) issue(std::integral_constant<int, 10>{});
      else if constexpr (g % 4 == 3) issue1(std::integral_constant<int, st_ + 3>{}, std::integral_constant<int, 0>{});
      else if constexpr (st_ >= 1) issue1(std::integral_constant<int, st_ + 2>{}, std::integral_constant<int, g % 4 + 2>{});
      acc[1] = MFMA(wf[g & 1][1], xf[g], acc[1]);
      if constexpr (g % 4 == 3 && g != 31) issue1(std::integral_constant<int, st_ + 3>{}, std::integral_constant<int, 1>{});
      acc[2] = MFMA(wf[g & 1][2], xf[g], acc[2]);
      if constexpr ((g & 3) == 0 && !NODOT) {
#pragma unroll
        for (int t = 0; t < 3; ++t) { cs[t] = sum8(wf[g & 1][t], cs[t]); pinf(cs[t]); }
      }
      if constexpr ((g & 1) == 0 && !NODOT) { s1 = sum8(xf[g], s1); s2 = sq8(xf[g], s2); pinf(s1); pinf(s2); }
      SBAR();
    });
    STAMP(2);
    bf16x8 qf[2];
    {
      LAS float* scs = (LAS float*)(L + SCR);
      LAS float* sst = (LAS float*)(L + SCR + 6144);
#pragma unroll
      for (int t = 0; t < 3; ++t) scs[((n * 3 + t) * 4 + m) * 64 + lane] = cs[t];
      const float a1 = s1 + swap32(s1), a2 = s2 + swap32(s2);
      sst[(m * 2 + n) * 64 + lane] = h ? a2 : a1;
      lds_bar();
      const float t1 = sst[(m * 2) * 64 + r32] + sst[(m * 2 + 1) * 64 + r32];
      const float t2 = sst[(m * 2) * 64 + 32 + r32] + sst[(m * 2 + 1) * 64 + 32 + r32];
      const float mean = t1 * (1.f / 512.f);
      const float var = __builtin_fmaxf(t2 * (1.f / 512.f) - mean * mean, 0.f);
      rstd = __builtin_amdgcn_rsqf(var + 1e-5f);
      mB = corr_m(-mean);
#pragma unroll
      for (int t = 2; t >= 0; --t) {
        const LAS float* p = scs + (n * 3 + t) * 256 + lane;
        const float c = (p[0] + p[64]) + (p[128] + p[192]);
        acc[t] = MFMA(corr_c(c), mB, acc[t]);
      }
      const unsigned kvg = (unsigned)(WS_KV + (size_t)(ib * 8 + idx) * 32768 + m * 8192 + n * 4096);
      const unsigned vpos = (h * 32 + (r32 ^ (4 * h))) * 16;
#pragma unroll
      for (int X = 0; X < 2; ++X) {
        const u32x4 kf = sc_frag(acc[1], X, rstd), vf = sc_frag(acc[2], X, rstd);
        if (ib < 15) {
          store_sc1(rs_ws, lane * 16, kvg + X * 2048, kf);
          store_sc1(rs_ws, vpos, kvg + X * 2048 + 1024, vf);
        }
        *(LAS u32x4*)(L + OWN + m * 8192 + n * 4096 + X * 2048 + lane * 16) = kf;
        *(LAS u32x4*)(L + OWN + m * 8192 + n * 4096 + X * 2048 + 1024 + vpos) = vf;
        qf[X] = __builtin_bit_cast(bf16x8, sc_frag(acc[0], X, rstd * (0.25f * 1.4426950408889634f)));
        pinv(qf[X]);
      }
    }
    STAMP(3);
    u32x4 nb[16];
    const int iw = w & 3;
    const int srcb = ib - 2 + (iw >> 1);
    if (act) {
      bf16x8 wh[2][2];
      float c2[2] = {0.f, 0.f};
      acc[0] = zero16; acc[1] = zero16;
      auto rdh = [&](auto G_) {
        constexpr int g = decltype(G_)::value;
        constexpr int base = ((g % 8) / 4) * 8192;
        wh[g & 1][0] = tr_frag(L + base + ag[0][g & 3], 512);
        wh[g & 1][1] = tr_frag(L + base + ag[1][g & 3], 512);
      };
      adv(ag[0], 2 * SLOT); adv(ag[1], 2 * SLOT);
      rdh(std::integral_constant<int, 0>{});
      static_for<0, 32>([&](auto G_) {
        constexpr int g = decltype(G_)::value;
        if constexpr (g == 7) {
          wait_bar<0>();
          issue(std::integral_constant<int, 11>{});
          if (w == 4 * hf && lane >= 1 && lane < 3 && cb < 16)
            __hip_atomic_store(fl + (size_t)(cb * 8 + idx) * 64 + 16 + (lane - 1), ep + 1, RLX_AGENT);
        }
        if constexpr (g == 23) wait_bar<0>();
        if constexpr (g % 8 == 7 && g < 31) {
          constexpr int d = (g == 7) ? -2 * SLOT : SLOT;
          adv(ag[0], d); adv(ag[1], d);
        }
        if constexpr (g + 1 < 32) rdh(std::integral_constant<int, g + 1>{});
        acc[0] = MFMA(wh[g & 1][0], xf[g], acc[0]);
        acc[1] = MFMA(wh[g & 1][1], xf[g], acc[1]);
        if constexpr ((g & 1) == 0) { c2[0] = sum8(wh[g & 1][0], c2[0]); c2[1] = sum8(wh[g & 1][1], c2[1]); pinf(c2[0]); pinf(c2[1]); }
        SBAR();
      });
      LAS float* scs = (LAS float*)(L + SCR);
      scs[((2 * n + 0) * 2 + (m & 1)) * 64 + lane] = c2[0];
      scs[((2 * n + 1) * 2 + (m & 1)) * 64 + lane] = c2[1];
      lds_bar();
#pragma unroll
      for (int t = 0; t < 2; ++t) {
        const LAS float* p = scs + (2 * n + t) * 128 + lane;
        acc[t] = MFMA(corr_c(p[0] + p[64]), mB, acc[t]);
      }
#pragma unroll
      for (int t = 0; t < 2; ++t) {
        const int ks = 4 * pairX + 2 * n + t;
        store_sc1(rs_ws, lane * 16, (unsigned)(WS_A2 + ((size_t)(ib * 4 + m) * 144 + ks) * 1024), h_frag(acc[t], rstd));
      }
    } else {
      wait_bar<0>();
      issue(std::integral_constant<int, 11>{});
      wait_bar<0>();
      if (srcb >= 0) {
        const u64 want = (((u64)(unsigned)__builtin_amdgcn_readfirstlane((unsigned)(ep >> 32))) << 32 | (unsigned)__builtin_amdgcn_readfirstlane((unsigned)ep)) + 1;
        GAS u64* f = fl + (size_t)(ib * 8 + idx) * 64 + 16 + (1 - (iw >> 1));
        for (unsigned spin = 0; spin < (1u << 22); ++spin) {
          const u64 v = __hip_atomic_load(f, RLX_AGENT);
          if (__builtin_amdgcn_readfirstlane((unsigned)(v == want))) break;
          __builtin_amdgcn_s_sleep(1);
        }
        __builtin_amdgcn_fence(__ATOMIC_ACQUIRE, "wavefront");
        const unsigned src = (unsigned)(WS_KV + (size_t)(srcb * 8 + idx) * 32768 + (iw & 1) * 16384);
#pragma unroll
        for (int j = 0; j < 16; ++j)
          nb[j] = __builtin_bit_cast(u32x4, __builtin_amdgcn_raw_buffer_load_b128(rs_ws, lane * 16, src + j * 1024, 16));
      }
      lds_bar();
      if (srcb >= 0) {
#pragma unroll
        for (int j = 0; j < 16; ++j) *(LAS u32x4*)(L + KVB + iw * 16384 + j * 1024 + lane * 16) = nb[j];
      }
      if (iw == 0) { u32x4 one = {0x3f803f80u, 0x3f803f80u, 0x3f803f80u, 0x3f803f80u}; *(LAS u32x4*)(L + lane * 16) = one; }
    }
    lds_bar();
    STAMP(4);
    if (w == 0 && lane == 0) __hip_atomic_store(fl + (size_t)(ib * 8 + idx) * 64, ep + 1, RLX_AGENT);
    {
      const int kt0 = 8 - (ib * 4 + m);
      const int t_lo = kt0 > 1 ? kt0 : 1;
      const lds_ptr kp = L + KVB + m * 8192 + n * 4096 + lane * 16;
      const int hs = tp & 1;
      const unsigned va = KVB + m * 8192 + n * 4096 + 1024 + (hs * 32 + ((4 * h + tq) ^ (4 * hs))) * 16 + (tp >> 1) * 8;
      const int qq = r32 - 4 * h;
      float mx[2] = {-INFINITY, -INFINITY};
      auto qk = [&](int t, int X, const f32x16& c) {
        const bf16x8 kf = *(LAS bf16x8*)(kp + t * 8192 + X * 2048);
        return MFMA(kf, qf[X], c);
      };
      auto mask = [&](f32x16& s, bool diag) {
#pragma unroll
        for (int r = 0; r < 16; ++r) {
          const bool le = ((r & 3) + 8 * (r >> 2)) <= qq;
          s[r] = (le == diag) ? s[r] : -INFINITY;
        }
      };
      auto rmax = [&](const f32x16& s, float a) {
#pragma unroll
        for (int r = 0; r < 16; r += 2) a = __builtin_fmaxf(__builtin_fmaxf(a, s[r]), s[r + 1]);
        return a;
      };
#pragma unroll
      for (int X = 0; X < 2; ++X) { f32x16 s = qk(8, X, zero16); mask(s, true); mx[X] = rmax(s, mx[X]); }
      if (kt0 <= 0) {
#pragma unroll
        for (int X = 0; X < 2; ++X) { f32x16 s = qk(0, X, zero16); mask(s, false); mx[X] = rmax(s, mx[X]); }
      }
#pragma unroll 1
      for (int t = t_lo; t < 8; ++t) {
#pragma unroll
        for (int X = 0; X < 2; ++X) { f32x16 s = qk(t, X, zero16); mx[X] = rmax(s, mx[X]); }
      }
      f32x16 nm[2], o[2] = {zero16, zero16};
#pragma unroll
      for (int X = 0; X < 2; ++X) {
        const float mm = -__builtin_fmaxf(mx[X], swap32(mx[X]));
#pragma unroll
        for (int r = 0; r < 16; ++r) nm[X][r] = mm;
      }
      auto pv = [&](int t, int X, f32x16& s) {
        u32x4 p0, p1;
#pragma unroll
        for (int j = 0; j < 4; ++j) {
          p0[j] = pk2(__builtin_amdgcn_exp2f(s[2 * j]), __builtin_amdgcn_exp2f(s[2 * j + 1]));
          p1[j] = pk2(__builtin_amdgcn_exp2f(s[8 + 2 * j]), __builtin_amdgcn_exp2f(s[8 + 2 * j + 1]));
        }
        const unsigned vb = blk ? 0u : va + t * 8192 + X * 2048;
        const bf16x8 v0 = tr_frag(L + vb, 128), v1 = tr_frag(L + vb + 256, 128);
        o[X] = MFMA(v0, __builtin_bit_cast(bf16x8, p0), o[X]);
        o[X] = MFMA(v1, __builtin_bit_cast(bf16x8, p1), o[X]);
      };
#pragma unroll
      for (int X = 0; X < 2; ++X) { f32x16 s = qk(8, X, nm[X]); mask(s, true); pv(8, X, s); }
      if (kt0 <= 0) {
#pragma unroll
        for (int X = 0; X < 2; ++X) { f32x16 s = qk(0, X, nm[X]); mask(s, false); pv(0, X, s); }
      }
#pragma unroll 1
      for (int t = t_lo; t < 8; ++t) {
#pragma unroll
        for (int X = 0; X < 2; ++X) { f32x16 s = qk(t, X, nm[X]); pv(t, X, s); }
      }
#pragma unroll
      for (int X = 0; X < 2; ++X) {
        const float inv = __builtin_amdgcn_rcpf(o[X][8]);
        const int ks = 112 + idx * 4 + 2 * n + X;
        store_sc1(rs_ws, lane * 16, (unsigned)(WS_A2 + ((size_t)(ib * 4 + m) * 144 + ks) * 1024), sc_frag(o[X], 0, inv));
      }
    }
    STAMP(5);
  }
  STAMP(11);
  STAMP_OUT(0);
}

__global__ __launch_bounds__(512, 2) void k2(const char* __restrict__ x, const char* __restrict__ wo,
                                             const char* __restrict__ wd, char* ws, char* __restrict__ out) {
  __shared__ __attribute__((aligned(1024))) char smem[131072];
  STAMP_DECL
  STAMP(0);
  const lds_ptr L = (lds_ptr)smem;
  const unsigned L0 = (unsigned)(size_t)L;
  const int tid = threadIdx.x, lane = tid & 63;
  const int w = __builtin_amdgcn_readfirstlane(tid >> 6);
  const int kq = w >> 1, rt = w & 1;
  const int b = blockIdx.x, xcd = b & 7, rr = b >> 3;
  const int p = xcd * 4 + (rr >> 3), c = rr & 7;
  const int h = lane >> 5, r32 = lane & 31;
  const int blk = (lane >> 4) & 1, tq = (lane & 15) >> 2, tp = lane & 3;
  constexpr int SLOT = 32768;
  const int rot2 = ROT2 ? (c + 2 * (p & 3)) % 9 : 0;
  const u32x4 xres = *(const u32x4*)(x + (size_t)(p * 64 + (tid >> 3)) * PX + c * 128 + (tid & 7) * 16);
  const unsigned vo_w = (lane >> 3) * PX + (((lane & 7) ^ (4 * ((lane >> 4) & 1))) * 16);
  const char* a2 = ws + WS_A2 + (size_t)(p * 2 + rt) * 144 * 1024;
  const unsigned vo_a = lane * 16;
  u32x4 af[4][4];
  auto issue1 = [&](auto C_, auto J_) {
    constexpr int cc = decltype(C_)::value, j = decltype(J_)::value;
    if constexpr (cc < 9) {
      int ca = cc + rot2; ca = ca >= 9 ? ca - 9 : ca;
      const int ks = ca < 6 ? 24 * kq + 4 * ca : ca == 6 ? 96 + 4 * kq : 112 + 8 * kq + 4 * (ca - 7);
      const char* wb = (ca < 7 ? wd + (size_t)ks * 16 * PX : wo + (size_t)(ks - 112) * 16 * PX) + c * 128 + (size_t)rt * 32 * PX;
      const unsigned dst = L0 + (cc & 3) * SLOT + kq * 8192 + rt * 4096;
      dma16(dst + j * 1024, vo_w, wb + (size_t)j * 8 * PX);
      gload1(af[cc & 3][j], vo_a, a2 + (size_t)(ks + j) * 1024);
    }
  };
  auto issue = [&](auto C_) { static_for<0, 4>([&](auto J_) { issue1(C_, J_); }); };
  auto pin = [&](auto C_) {
    constexpr int cc = decltype(C_)::value;
    pin4(af[cc & 3]);
  };
  unsigned aw[2];
#pragma unroll
  for (int t = 0; t < 2; ++t)
    aw[t] = kq * 8192 + (4 * h + tq) * 128 + (((4 * t + 2 * blk + (tp >> 1)) ^ (4 * ((tq >> 1) & 1))) * 16) + (tp & 1) * 8;
  const f32x16 zero16 = {0, 0, 0, 0, 0, 0, 0, 0, 0, 0, 0, 0, 0, 0, 0, 0};
  f32x16 acc[2] = {zero16, zero16};
  bf16x8 wf[2][2];
  auto rd = [&](auto G_) {
    constexpr int g = decltype(G_)::value;
    constexpr int base = ((g / 4) & 3) * SLOT + (g & 3) * 2048;
    wf[g & 1][0] = tr_frag(L + base + aw[0], 1024);
    wf[g & 1][1] = tr_frag(L + base + aw[1], 1024);
  };
  static_for<0, 4>(issue);
  wait_bar<24>();
  pin(std::integral_constant<int, 0>{});
  STAMP(1);
  rd(std::integral_constant<int, 0>{});
  static_for<0, 36>([&](auto G_) {
    constexpr int g = decltype(G_)::value;
    constexpr int cc = g / 4;
    if constexpr ((g & 3) == 3 && cc + 1 < 9) {
      constexpr int hi = cc + 3 < 8 ? cc + 3 : 8;
      wait_bar<8 * (hi - (cc + 1))>();
      pin(std::integral_constant<int, cc + 1>{});
      STAMP(2 + cc);
    }
    if constexpr (g + 1 < 36) rd(std::integral_constant<int, g + 1>{});
    const bf16x8 a = __builtin_bit_cast(bf16x8, af[cc & 3][g & 3]);
    acc[0] = MFMA(wf[g & 1][0], a, acc[0]);
    if constexpr ((g & 3) == 3) issue1(std::integral_constant<int, cc + 4>{}, std::integral_constant<int, 0>{});
    else if constexpr (cc >= 1) issue1(std::integral_constant<int, cc + 3>{}, std::integral_constant<int, (g & 3) + 1>{});
    acc[1] = MFMA(wf[g & 1][1], a, acc[1]);
    SBAR();
  });
  STAMP(10);
  lds_bar();
  constexpr int PP = 272;
#pragma unroll
  for (int t = 0; t < 2; ++t)
#pragma unroll
    for (int g4 = 0; g4 < 4; ++g4) {
      f32x4 v = {acc[t][4 * g4], acc[t][4 * g4 + 1], acc[t][4 * g4 + 2], acc[t][4 * g4 + 3]};
      *(LAS f32x4*)(L + kq * 64 * PP + (rt * 32 + r32) * PP + (t * 32 + 8 * g4 + 4 * h) * 4) = v;
    }
  lds_bar();
  {
    const lds_ptr q = L + (tid >> 3) * PP + (tid & 7) * 32;
    f32x4 lo = *(LAS f32x4*)q, hi = *(LAS f32x4*)(q + 16);
#pragma unroll
    for (int k = 1; k < 4; ++k) { lo += *(LAS f32x4*)(q + k * 64 * PP); hi += *(LAS f32x4*)(q + k * 64 * PP + 16); }
    u32x4 o;
#pragma unroll
    for (int j = 0; j < 2; ++j) {
      o[j] = pk2(lo[2 * j] + __uint_as_float(xres[j] << 16), lo[2 * j + 1] + __uint_as_float(xres[j] & 0xffff0000u));
      o[2 + j] = pk2(hi[2 * j] + __uint_as_float(xres[2 + j] << 16), hi[2 * j + 1] + __uint_as_float(xres[2 + j] & 0xffff0000u));
    }
    *(u32x4*)(out + (size_t)(p * 64 + (tid >> 3)) * PX + c * 128 + (tid & 7) * 16) = o;
  }
  STAMP(11);
  STAMP_OUT(1);
}

extern "C" void kernel_launch(void* const* d_in, const int* in_sizes, int n_in, void* d_out, int out_size,
                              void* d_ws, size_t ws_size, hipStream_t stream) {
  const char* x = (const char*)d_in[0];
  const char* wqkv = (const char*)d_in[1];
  const char* wo = (const char*)d_in[2];
  const char* wg = (const char*)d_in[4];
  const char* wu = (const char*)d_in[5];
  const char* wd = (const char*)d_in[6];
  k1<<<256, 512, 0, stream>>>(x, wqkv, wg, wu, (char*)d_ws);
  k2<<<256, 512, 0, stream>>>(x, wo, wd, (char*)d_ws, (char*)d_out);
}
```

```cpp
#include <hip/hip_runtime.h>
#include <hip/hip_bf16.h>
#include <stdint.h>
#include <type_traits>

typedef unsigned long long u64;
typedef __attribute__((ext_vector_type(8))) __bf16 bf16x8;
typedef __attribute__((ext_vector_type(2))) __bf16 bf16x2;
typedef __attribute__((ext_vector_type(4))) short s16x4;
typedef __attribute__((ext_vector_type(16))) float f32x16;
typedef __attribute__((ext_vector_type(4))) float f32x4;
typedef __attribute__((ext_vector_type(4))) unsigned u32x4;
#define LAS __attribute__((address_space(3)))
#define GAS __attribute__((address_space(1)))
#define DI __device__ __forceinline__
typedef LAS char* lds_ptr;
template <int V> using IC = std::integral_constant<int, V>;

#define MFMA(a, b, c) __builtin_amdgcn_mfma_f32_32x32x16_bf16((a), (b), (c), 0, 0, 0)
#define MFMA16(a, b, c) __builtin_amdgcn_mfma_f32_16x16x32_bf16((a), (b), (c), 0, 0, 0)
#define RLX_AGENT __ATOMIC_RELAXED, __HIP_MEMORY_SCOPE_AGENT
#define SBAR() __builtin_amdgcn_sched_barrier(0)

#define TOUCH 1
#define ROT1 ((ib + 2 * (rr & 3)) & 7)
constexpr int PX = 1024;
constexpr int PQ = 3072;
constexpr int PF = 3584;
constexpr size_t WS_A2 = 0;
constexpr size_t WS_KV = 9437184;
constexpr size_t WS_Q  = WS_KV + 4194304;
constexpr size_t WS_FL = WS_Q + 1048576;
constexpr size_t WS_EP = WS_FL + 131072;
constexpr size_t WS_ST = WS_EP + 4096;
constexpr int KVB = 98304, OWN = 131072, QB = 147456, SCR = 155648, SST = SCR + 4096, ONES = SCR + 6144;

template <int I, int N, class F>
DI void static_for(F&& f) {
  if constexpr (I < N) { f(IC<I>{}); static_for<I + 1, N>(f); }
}
DI void dma16(unsigned lds_dst, unsigned voff, const void* sbase) {
  asm volatile("s_mov_b32 m0, %0\n\ts_nop 0\n\tglobal_load_lds_dwordx4 %1, %2" ::"s"(lds_dst), "v"(voff), "s"(sbase) : "memory");
}
DI void dma4(unsigned lds_dst, unsigned voff, const void* sbase) {
  asm volatile("s_mov_b32 m0, %0\n\ts_nop 0\n\tglobal_load_lds_dword %1, %2" ::"s"(lds_dst), "v"(voff), "s"(sbase) : "memory");
}
template <int N>
DI void wait_bar() { asm volatile("s_waitcnt vmcnt(%0) lgkmcnt(0)\n\ts_barrier" ::"n"(N) : "memory"); }
template <int N>
DI void wait_vm() { asm volatile("s_waitcnt vmcnt(%0)" ::"n"(N) : "memory"); }
DI void lds_bar() { asm volatile("s_waitcnt lgkmcnt(0)\n\ts_barrier" ::: "memory"); }
DI void gload_sc1(u32x4& a, unsigned voff, const void* sbase) {
  asm volatile("global_load_dwordx4 %0, %1, %2 sc1" : "=&v"(a) : "v"(voff), "s"(sbase) : "memory");
}
DI void pin4(u32x4 (&a)[4]) { asm volatile("" : "+v"(a[0]), "+v"(a[1]), "+v"(a[2]), "+v"(a[3])); }
DI void pinu(unsigned& x) { asm volatile("" : "+v"(x)); }
DI void pinq(f32x4& x) { asm volatile("" : "+v"(x)); }
template <int N>
DI void adv(unsigned (&a)[N], int d) {
#pragma unroll
  for (int j = 0; j < N; ++j) { a[j] += d; pinu(a[j]); }
}
DI bf16x8 tr_frag(lds_ptr p, int off1) {
  s16x4 a = __builtin_amdgcn_ds_read_tr16_b64_v4i16((LAS s16x4*)p);
  s16x4 b = __builtin_amdgcn_ds_read_tr16_b64_v4i16((LAS s16x4*)(p + off1));
  return __builtin_bit_cast(bf16x8, __builtin_shufflevector(a, b, 0, 1, 2, 3, 4, 5, 6, 7));
}
DI unsigned pk2(float a, float b) { bf16x2 v = {(__bf16)a, (__bf16)b}; return __builtin_bit_cast(unsigned, v); }
DI void stat2(float (&s)[4], unsigned dw) {
  float t0, t1;
  asm volatile("v_lshlrev_b32 %4, 16, %6\n\tv_and_b32 %5, 0xffff0000, %6\n\tv_add_f32 %0, %0, %4\n\tv_add_f32 %1, %1, %5\n\t"
               "v_fmac_f32 %2, %4, %4\n\tv_fmac_f32 %3, %5, %5"
               : "+v"(s[0]), "+v"(s[1]), "+v"(s[2]), "+v"(s[3]), "=&v"(t0), "=&v"(t1) : "v"(dw));
}
DI float swap32(float v) {
  auto r = __builtin_amdgcn_permlane32_swap(__float_as_uint(v), __float_as_uint(v), false, false);
  return (threadIdx.x & 32) ? __uint_as_float(r[0]) : __uint_as_float(r[1]);
}
DI void split3(float c, unsigned& t1, unsigned& t2, unsigned& t3) {
  t1 = __float_as_uint(c) & 0xffff0000u;
  float r = c - __uint_as_float(t1);
  t2 = __float_as_uint(r) & 0xffff0000u;
  float r2 = r - __uint_as_float(t2);
  t3 = __float_as_uint(r2) & 0xffff0000u;
}
DI unsigned hi2(unsigned lo, unsigned hi) { return (lo >> 16) | hi; }
DI bf16x8 corr_c(float c) {
  unsigned a, b, d; split3(c, a, b, d);
  u32x4 u = {hi2(a, a), hi2(b, b), hi2(a, d), hi2(b, d)};
  return __builtin_bit_cast(bf16x8, u);
}
DI bf16x8 corr_m(float m) {
  unsigned a, b, d; split3(m, a, b, d);
  u32x4 u = {hi2(a, b), hi2(a, b), hi2(d, a), hi2(d, b)};
  return __builtin_bit_cast(bf16x8, u);
}
DI float geglu(float g, float u) {
  const float c1 = -2.0f * 1.4426950408889634f * 0.7978845608028654f, c2 = c1 * 0.044715f;
  float e = __builtin_amdgcn_exp2f(g * __builtin_fmaf(g * g, c2, c1));
  return g * u * __builtin_amdgcn_rcpf(1.0f + e);
}
DI void store_sc1(__amdgpu_buffer_rsrc_t rs, unsigned off, u32x4 v) {
  __builtin_amdgcn_raw_buffer_store_b128(v, rs, off, 0, 16);
}
DI u32x4 load_sc1(__amdgpu_buffer_rsrc_t rs, unsigned off) {
  return __builtin_bit_cast(u32x4, __builtin_amdgcn_raw_buffer_load_b128(rs, off, 0, 16));
}
DI u32x4 h_frag(const f32x16& a, float rstd) {
  float hv[8];
#pragma unroll
  for (int r = 0; r < 8; ++r) hv[r] = geglu(a[r] * rstd, a[r + 8] * rstd);
  u32x4 o = {pk2(hv[0], hv[1]), pk2(hv[2], hv[3]), pk2(hv[4], hv[5]), pk2(hv[6], hv[7])};
  return o;
}
DI u32x4 sc_frag(const f32x16& a, int X, float s) {
  u32x4 o = {pk2(a[8 * X] * s, a[8 * X + 1] * s), pk2(a[8 * X + 2] * s, a[8 * X + 3] * s),
             pk2(a[8 * X + 4] * s, a[8 * X + 5] * s), pk2(a[8 * X + 6] * s, a[8 * X + 7] * s)};
  return o;
}
DI void wait_flags(GAS u64* p, bool use, u64 tag) {
#pragma unroll 1
  for (unsigned spin = 0; spin < (1u << 17); ++spin) {
    const u64 v = __hip_atomic_load(p, RLX_AGENT);
    if (__all(!use || v == tag)) break;
    __builtin_amdgcn_s_sleep(1);
  }
  __builtin_amdgcn_fence(__ATOMIC_ACQUIRE, "wavefront");
}

#define STAMP(k)
#define STAMP_DECL
#define STAMP_OUT(kid)

__global__ __launch_bounds__(512, 2) void fused(const char* __restrict__ x, const char* __restrict__ wqkv,
                                                const char* __restrict__ wo, const char* __restrict__ wg,
                                                const char* __restrict__ wu, const char* __restrict__ wd,
                                                char* ws, char* __restrict__ out) {
  __shared__ __attribute__((aligned(1024))) char smem[163840];
  STAMP_DECL
  STAMP(0);
  const lds_ptr L = (lds_ptr)smem;
  const unsigned L0 = (unsigned)(size_t)L;
  const int tid = threadIdx.x, lane = tid & 63;
  const int w = __builtin_amdgcn_readfirstlane(tid >> 6);
  const int m = w >> 1, n = w & 1;
  const int b = blockIdx.x, xcd = b & 7, rr = b >> 3;
  const int ib = (xcd >> 2) * 8 + (rr >> 2);
  const int idx = 2 * (xcd & 3) + (rr & 1);
  const int tyB = (rr >> 1) & 1;
  const int h = lane >> 5, r32 = lane & 31;
  const int blk = (lane >> 4) & 1, tq = (lane & 15) >> 2, tp = lane & 3;
  const int perm = m ^ n;
  const int rot = ROT1;
  auto kc = [&](int c) { return (c + rot) & 7; };

  GAS u64* const fl = (GAS u64*)(ws + WS_FL);
  u64 tag;
  if (w == 0) dma16(L0 + SCR + 3072, 0u, ws + WS_EP);
  auto get_tag = [&]() {
    const u64 e = *(LAS u64*)(L + SCR + 3072);
    tag = ((u64)(unsigned)__builtin_amdgcn_readfirstlane((unsigned)(e >> 32)) << 32 | (unsigned)__builtin_amdgcn_readfirstlane((unsigned)e)) + 1;
  };
  GAS u64* const myfl = fl + (size_t)(ib * 16 + tyB * 8 + idx) * 64 + w;
  auto publish = [&](int ev) { if (lane == 0) __hip_atomic_store(myfl + 8 * ev, tag, RLX_AGENT); };
  const __amdgpu_buffer_rsrc_t rs_ws = __builtin_amdgcn_make_buffer_rsrc(ws, 0, (int)WS_FL, 0x00020000);

  const unsigned vo_x = (lane >> 3) * PX + (((lane & 7) ^ ((4 * (w & 1) + (lane >> 4)) & 7)) * 16);
  const unsigned sw_w = (lane & 7) ^ (4 * ((lane >> 4) & 1));
  const unsigned vo_g = (lane >> 3) * PF + sw_w * 16;
  const unsigned vo_u = (lane >> 3) * PF + (sw_w ^ 2) * 16;
  const char* xb = x + (size_t)(ib * 128 + 8 * w) * PX;
  const char* gb = wg + (size_t)(8 * w) * PF;
  const char* ub = wu + (size_t)(8 * w) * PF;
  auto issue_pair = [&](unsigned dst, int P, int q4, int j) {
    const int c = kc(2 * q4 + (j >> 1));
    if (j & 1) dma16(dst + 16384 + (j >> 1) * 8192, vo_u, ub + c * 64 * PF + P * 128);
    else       dma16(dst + (j >> 1) * 8192, vo_g, gb + c * 64 * PF + P * 128);
  };

  auto touch_w2 = [&]() {
    if (w == 1) dma4(L0 + SCR + 7168, (lane < 56 ? lane : 0) * 128, wd + (size_t)b * 56 * 128);
    if (w == 2) dma4(L0 + SCR + 7168, (lane & 15) * 128, wo + (size_t)b * 16 * 128);
  };
  unsigned ax[4], ag[2][4];
#pragma unroll
  for (int s = 0; s < 4; ++s) {
    const int sa = s ^ perm;
    ax[s] = r32 * 128 + (((2 * sa + h) ^ ((r32 >> 1) & 7)) * 16);
#pragma unroll
    for (int t = 0; t < 2; ++t)
      ag[t][s] = (16 * sa + 8 * h + tq) * 128 + (((2 * (2 * n + t) + (tp >> 1)) ^ (4 * ((tq >> 1) & 1)) ^ (blk * 2)) * 16) + (tp & 1) * 8 + blk * 16384;
  }
  const unsigned selw = ((lane & 15) == ((lane >> 4) & 1)) ? 0x3f803f80u : 0u;
  const bf16x8 sel = __builtin_bit_cast(bf16x8, (u32x4){selw, selw, selw, selw});
  if (w == 7 && lane < 32) *(LAS u32x4*)(L + ONES + lane * 16) = (u32x4){0x3f803f80u, 0x3f803f80u, 0x3f803f80u, 0x3f803f80u};

  bf16x8 xf[32];
  float sx[4] = {0.f, 0.f, 0.f, 0.f};
  float rstd;
  bf16x8 mB;
  const f32x16 zero16 = {0, 0, 0, 0, 0, 0, 0, 0, 0, 0, 0, 0, 0, 0, 0, 0};
  const f32x4 zero4 = {0, 0, 0, 0};
  LAS float* const scs = (LAS float*)(L + SCR);
  LAS float* const sst = (LAS float*)(L + SST);
  auto put_cs = [&](int tile, int part, const f32x4& c) {
    if ((lane & 15) < 2) *(LAS f32x4*)(scs + (tile * 4 + part) * 32 + (lane & 15) * 16 + (lane >> 4) * 4) = c;
  };
  auto put_stats = [&]() {
    const float a1 = sx[0] + sx[1], a2 = sx[2] + sx[3];
    const float b1 = a1 + swap32(a1), b2 = a2 + swap32(a2);
    sst[(m * 2 + n) * 64 + lane] = h ? b2 : b1;
  };
  auto get_stats = [&]() {
    const float t1 = sst[(m * 2) * 64 + r32] + sst[(m * 2 + 1) * 64 + r32];
    const float t2 = sst[(m * 2) * 64 + 32 + r32] + sst[(m * 2 + 1) * 64 + 32 + r32];
    const float mean = t1 * (1.f / 512.f);
    const float var = __builtin_fmaxf(t2 * (1.f / 512.f) - mean * mean, 0.f);
    rstd = __builtin_amdgcn_rsqf(var + 1e-5f);
    mB = corr_m(-0.5f * mean);
  };
  auto stats_step = [&](auto G_) {
    constexpr int g = decltype(G_)::value;
    const u32x4 u = __builtin_bit_cast(u32x4, xf[g & ~1]);
    stat2(sx, u[2 * (g & 1)]); stat2(sx, u[2 * (g & 1) + 1]);
  };
  const unsigned a2row = (unsigned)(WS_A2 + (size_t)(ib * 4 + m) * 144 * 1024) + lane * 16;
  bf16x8 wf[2][3];
  f32x16 acc[3];
  f32x4 cs[3];
  auto rdp = [&](auto G_, int base) {
    constexpr int g = decltype(G_)::value;
    wf[g & 1][0] = tr_frag(L + base + ag[0][g & 3], 512);
    wf[g & 1][1] = tr_frag(L + base + ag[1][g & 3], 512);
  };
  auto pair_epilogue = [&](int P, int nparts) {
#pragma unroll
    for (int t = 0; t < 2; ++t) {
      const LAS float* p = scs + (2 * n + t) * 128 + r32;
      const float c = nparts == 4 ? (p[0] + p[32]) + (p[64] + p[96]) : p[0] + p[32];
      acc[t] = MFMA(corr_c(c), mB, acc[t]);
    }
#pragma unroll
    for (int t = 0; t < 2; ++t) store_sc1(rs_ws, a2row + (4 * P + 2 * n + t) * 1024, h_frag(acc[t], rstd));
  };

  if (tyB) {
    constexpr int SLOT = 32768;
    const int P0 = 8 + 2 * idx, PH = 24 + (idx >> 1);
    const bool act = (w >> 2) == (idx & 1);
    auto issue1 = [&](auto S_, auto J_) {
      constexpr int s = decltype(S_)::value, j = decltype(J_)::value;
      const unsigned dst = L0 + (s & 3) * SLOT + w * 1024;
      if constexpr (s < 8) {
        const int c = kc(s);
        if constexpr (j == 0) dma16(dst + 8192, vo_x, xb + c * 128);
        if constexpr (j == 1) dma16(dst + 24576, vo_x, xb + c * 128 + 64 * PX);
        if constexpr (j == 2) dma16(dst, vo_g, gb + c * 64 * PF + P0 * 128);
        if constexpr (j == 3) dma16(dst + 16384, vo_u, ub + c * 64 * PF + P0 * 128);
      } else if constexpr (s < 16) {
        issue_pair(dst, s < 12 ? P0 + 1 : PH, (s - 8) & 3, j);
      }
    };
    auto issue = [&](auto S_) { static_for<0, 4>([&](auto J_) { issue1(S_, J_); }); };
    auto rd = [&](auto G_) {
      constexpr int g = decltype(G_)::value;
      constexpr int st_ = g < 32 ? g / 4 : 8 + (g - 32) / 8;
      constexpr int sub = g < 32 ? 0 : ((g - 32) % 8) / 4;
      rdp(G_, (st_ & 1) * SLOT + sub * 8192);
      if constexpr (g < 32) xf[g] = *(LAS bf16x8*)(L + (st_ & 1) * SLOT + ax[g & 3]);
    };
    adv(ax, 8192 + (m & 1) * 4096 + (m >> 1) * 16384);
    acc[0] = zero16; acc[1] = zero16; cs[0] = zero4; cs[1] = zero4;
    static_for<0, 4>(issue);
    wait_bar<12>();
    get_tag();
    touch_w2();
    STAMP(1);
    rd(IC<0>{});
    static_for<0, 64>([&](auto G_) {
      constexpr int g = decltype(G_)::value;
      constexpr int st_ = g < 32 ? g / 4 : 8 + (g - 32) / 8;
      constexpr int pos = g < 32 ? g % 4 : (g - 32) % 8;
      constexpr bool last = g < 32 ? (pos == 3) : (pos == 7);
      if constexpr (last) {
        if constexpr (st_ == 8) { wait_bar<3>(); publish(1); } else wait_bar<8>();
        if constexpr (st_ & 1) {
          constexpr int d = (st_ & 2) ? -65536 : 65536;
          adv(ag[0], d); adv(ag[1], d);
          if constexpr (st_ < 7) adv(ax, d);
        }
      }
      if constexpr (g + 1 < 64) rd(IC<g + 1>{});
      SBAR();
      acc[0] = MFMA(wf[g & 1][0], xf[g & 31], acc[0]);
      if constexpr (g == 63) issue(IC<15>{});
      else if constexpr (last) issue1(IC<st_ + 4>{}, IC<0>{});
      else if constexpr (st_ >= 1 && g < 32) issue1(IC<st_ + 3>{}, IC<pos + 1>{});
      else if constexpr (g >= 32 && (pos & 1)) issue1(IC<st_ + 3>{}, IC<(pos + 1) / 2>{});
      acc[1] = MFMA(wf[g & 1][1], xf[g & 31], acc[1]);
      if constexpr ((g & 3) == 0) { cs[0] = MFMA16(wf[g & 1][0], sel, cs[0]); cs[1] = MFMA16(wf[g & 1][1], sel, cs[1]); }
      if constexpr (g < 32) stats_step(G_);
      SBAR();
      if constexpr ((g & 31) == 31) {
        STAMP(2 + 2 * (g / 32));
        put_cs(2 * n, m, cs[0]); put_cs(2 * n + 1, m, cs[1]);
        if constexpr (g == 31) put_stats();
        lds_bar();
        if constexpr (g == 31) get_stats();
        pair_epilogue(P0 + g / 32, 4);
        acc[0] = zero16; acc[1] = zero16; cs[0] = zero4; cs[1] = zero4;
        STAMP(3 + 2 * (g / 32));
      }
    });
    if (act) {
      rd(IC<64>{});
      static_for<64, 96>([&](auto G_) {
        constexpr int g = decltype(G_)::value;
        if constexpr (g == 71) { wait_bar<0>(); publish(2); }
        if constexpr (g == 79) { adv(ag[0], 65536); adv(ag[1], 65536); }
        if constexpr (g + 1 < 96) rd(IC<g + 1>{});
        SBAR();
        acc[0] = MFMA(wf[g & 1][0], xf[g & 31], acc[0]);
        acc[1] = MFMA(wf[g & 1][1], xf[g & 31], acc[1]);
        if constexpr ((g & 1) == 0) { cs[0] = MFMA16(wf[g & 1][0], sel, cs[0]); cs[1] = MFMA16(wf[g & 1][1], sel, cs[1]); }
        SBAR();
      });
      put_cs(2 * n, m & 1, cs[0]); put_cs(2 * n + 1, m & 1, cs[1]);
      lds_bar();
      pair_epilogue(PH, 2);
    } else {
      wait_bar<0>(); publish(2);
      const int iw = w & 3;
      const int sb = ib - 2 + (lane < 3 ? lane : 2);
      wait_flags(fl + (size_t)((sb < 0 ? 0 : sb) * 16 + idx) * 64 + 2 * iw + 1, lane < 3 && sb >= 0, tag);
      u32x4 nb[14];
#pragma unroll
      for (int j = 0; j < 3; ++j) {
        const int s2 = ib - 2 + j < 0 ? 0 : ib - 2 + j;
#pragma unroll
        for (int k = 0; k < 4; ++k)
          nb[j * 4 + k] = load_sc1(rs_ws, (unsigned)(WS_KV + (size_t)(s2 * 8 + idx) * 32768 + 16384 + iw * 4096 + k * 1024) + lane * 16);
      }
#pragma unroll
      for (int k = 0; k < 2; ++k) nb[12 + k] = load_sc1(rs_ws, (unsigned)(WS_Q + (size_t)(ib * 8 + idx) * 8192 + iw * 2048 + k * 1024) + lane * 16);
#pragma unroll
      for (int k = 0; k < 4; ++k) *(LAS u32x4*)(L + OWN + iw * 4096 + k * 1024 + lane * 16) = nb[8 + k];
#pragma unroll
      for (int k = 0; k < 2; ++k) *(LAS u32x4*)(L + QB + iw * 2048 + k * 1024 + lane * 16) = nb[12 + k];
      lds_bar();
#pragma unroll
      for (int j = 0; j < 2; ++j)
#pragma unroll
        for (int k = 0; k < 4; ++k) *(LAS u32x4*)(L + KVB + (j * 4 + iw) * 4096 + k * 1024 + lane * 16) = nb[j * 4 + k];
    }
  } else {
    constexpr int SLOT = 40960;
    const unsigned vo_q = (lane >> 3) * PQ + sw_w * 16;
    const char* qb = wqkv + (size_t)(8 * w) * PQ + idx * 128;
    auto issue1 = [&](auto S_, auto J_) {
      constexpr int s = decltype(S_)::value, j = decltype(J_)::value;
      const unsigned dst = L0 + (s % 3) * SLOT + w * 1024;
      if constexpr (s < 8) {
        const int c = kc(s);
        if constexpr (j == 0) dma16(dst, vo_x, xb + c * 128);
        if constexpr (j == 1) dma16(dst + 8192, vo_x, xb + c * 128 + 64 * PX);
        if constexpr (j >= 2) dma16(dst + j * 8192, vo_q, qb + c * 64 * PQ + (j - 2) * 1024);
      } else if constexpr (j < 4) {
        issue_pair(dst, idx, s - 8, j);
      }
    };
    auto issue = [&](auto S_) { static_for<0, 5>([&](auto J_) { issue1(S_, J_); }); };
    unsigned aq[4];
#pragma unroll
    for (int s = 0; s < 4; ++s)
      aq[s] = (16 * (s ^ perm) + 8 * h + tq) * 128 + (((4 * n + 2 * blk + (tp >> 1)) ^ (4 * ((tq >> 1) & 1))) * 16) + (tp & 1) * 8;
    auto rd = [&](auto G_) {
      constexpr int g = decltype(G_)::value;
#pragma unroll
      for (int t = 0; t < 3; ++t) wf[g & 1][t] = tr_frag(L + 16384 + t * 8192 + aq[g & 3], 512);
      xf[g] = *(LAS bf16x8*)(L + ax[g & 3]);
    };
    adv(ax, m * 4096);
#pragma unroll
    for (int t = 0; t < 3; ++t) { acc[t] = zero16; cs[t] = zero4; }
    static_for<0, 3>(issue);
    wait_bar<10>();
    get_tag();
    touch_w2();
    STAMP(1);
    rd(IC<0>{});
    static_for<0, 32>([&](auto G_) {
      constexpr int g = decltype(G_)::value;
      constexpr int st_ = g / 4;
      if constexpr (g % 4 == 3) {
        wait_bar<(st_ + 2 < 8 ? 5 : 4)>();
        if constexpr (st_ < 7) {
          constexpr int d = (st_ % 3 == 2) ? -2 * SLOT : SLOT;
          adv(aq, d); adv(ax, d);
        }
      }
      if constexpr (g + 1 < 32) rd(IC<g + 1>{});
      SBAR();
      acc[0] = MFMA(wf[g & 1][0], xf[g], acc[0]);
      if constexpr (g % 4 == 3) issue1(IC<st_ + 3>{}, IC<0>{});
      else if constexpr (st_ >= 1) issue1(IC<st_ + 2>{}, IC<g % 4 + 2>{});
      acc[1] = MFMA(wf[g & 1][1], xf[g], acc[1]);
      if constexpr (g % 4 == 3) issue1(IC<st_ + 3>{}, IC<1>{});
      acc[2] = MFMA(wf[g & 1][2], xf[g], acc[2]);
      if constexpr ((g & 3) == 0) {
#pragma unroll
        for (int t = 0; t < 3; ++t) cs[t] = MFMA16(wf[g & 1][t], sel, cs[t]);
      }
      stats_step(G_);
      SBAR();
    });
    STAMP(2);
    {
#pragma unroll
      for (int t = 0; t < 3; ++t) put_cs(n * 3 + t, m, cs[t]);
      put_stats();
      lds_bar();
      get_stats();
#pragma unroll
      for (int t = 2; t >= 0; --t) {
        const LAS float* p = scs + (n * 3 + t) * 128 + r32;
        acc[t] = MFMA(corr_c((p[0] + p[32]) + (p[64] + p[96])), mB, acc[t]);
      }
      const unsigned kvg = (unsigned)(WS_KV + (size_t)(ib * 8 + idx) * 32768 + n * 16384 + m * 4096);
      const unsigned vpos = (h * 32 + (r32 ^ (4 * h))) * 16;
#pragma unroll
      for (int X = 0; X < 2; ++X) {
        const u32x4 kf = sc_frag(acc[1], X, rstd), vf = sc_frag(acc[2], X, rstd);
        const u32x4 qf = sc_frag(acc[0], X, rstd * (0.25f * 1.4426950408889634f));
        store_sc1(rs_ws, kvg + X * 2048 + lane * 16, kf);
        store_sc1(rs_ws, kvg + X * 2048 + 1024 + vpos, vf);
        if (n == 0) {
          *(LAS u32x4*)(L + OWN + m * 4096 + X * 2048 + lane * 16) = kf;
          *(LAS u32x4*)(L + OWN + m * 4096 + X * 2048 + 1024 + vpos) = vf;
          *(LAS u32x4*)(L + QB + m * 2048 + X * 1024 + lane * 16) = qf;
        } else {
          store_sc1(rs_ws, (unsigned)(WS_Q + (size_t)(ib * 8 + idx) * 8192 + m * 2048 + X * 1024) + lane * 16, qf);
        }
      }
    }
    STAMP(3);
    acc[0] = zero16; acc[1] = zero16; cs[0] = zero4; cs[1] = zero4;
    adv(ag[0], 2 * SLOT); adv(ag[1], 2 * SLOT);
    u32x4 nb[4];
    const int sb = ib - 2 + (w >> 2);
    rdp(IC<0>{}, 0);
    static_for<0, 32>([&](auto G_) {
      constexpr int g = decltype(G_)::value;
      if constexpr (g == 7) { wait_bar<2>(); publish(0); }
      if constexpr (g == 15) wait_bar<4>();
      if constexpr (g == 23) {
        wait_bar<0>();
        wait_flags(fl + (size_t)((sb < 0 ? 0 : sb) * 16 + idx) * 64 + 2 * (w & 3), sb >= 0, tag);
#pragma unroll
        for (int k = 0; k < 4; ++k)
          nb[k] = load_sc1(rs_ws, (unsigned)(WS_KV + (size_t)((sb < 0 ? 0 : sb) * 8 + idx) * 32768 + (w & 3) * 4096 + k * 1024) + lane * 16);
      }
      if constexpr (g % 8 == 7 && g < 31) {
        constexpr int d = (g == 7) ? -2 * SLOT : SLOT;
        adv(ag[0], d); adv(ag[1], d);
      }
      if constexpr (g + 1 < 32) rdp(IC<g + 1>{}, (((g + 1) % 8) / 4) * 8192);
      SBAR();
      acc[0] = MFMA(wf[g & 1][0], xf[g], acc[0]);
      if constexpr (g == 1 || g == 3) issue1(IC<10>{}, IC<2 + g / 2>{});
      if constexpr (g >= 7 && g <= 13 && (g & 1)) issue1(IC<11>{}, IC<(g - 7) / 2>{});
      acc[1] = MFMA(wf[g & 1][1], xf[g], acc[1]);
      if constexpr ((g & 3) == 0) { cs[0] = MFMA16(wf[g & 1][0], sel, cs[0]); cs[1] = MFMA16(wf[g & 1][1], sel, cs[1]); }
      SBAR();
    });
    STAMP(4);
    put_cs(2 * n, m, cs[0]); put_cs(2 * n + 1, m, cs[1]);
    lds_bar();
#pragma unroll
    for (int k = 0; k < 4; ++k) *(LAS u32x4*)(L + KVB + w * 4096 + k * 1024 + lane * 16) = nb[k];
    pair_epilogue(idx, 4);
    STAMP(5);
  }
  constexpr int SLOT2 = 32768;
  const int kq = w >> 1, rt = w & 1;
  const int p = xcd * 4 + (rr >> 3), c = rr & 7;
  const int RT = p * 2 + rt, pi = RT >> 2, pm = RT & 3;
  const unsigned vo_w = (lane >> 3) * PX + sw_w * 16;
  const unsigned a2c = (unsigned)(WS_A2 + (size_t)RT * 144 * 1024) + lane * 16;
  const char* wdb = wd + c * 128 + (size_t)rt * 32 * PX;
  const char* wob = wo + c * 128 + (size_t)rt * 32 * PX;
  int fw;
  {
    const int j = lane < 22 ? lane : 0, ch = j >> 1;
    int wgp, ev;
    if (ch < 4) { wgp = 8 + 2 * kq + (ch & 1); ev = 1 + (ch >> 1); }
    else if (ch < 6) { wgp = 2 * kq + (ch & 1); ev = 3; }
    else if (ch == 6) { wgp = 8 + 2 * kq + (pm >> 1); ev = 3; }
    else { wgp = ((ch - 7) & 1) * 8 + 2 * kq + ((ch - 7) >> 1); ev = 4; }
    fw = (pi * 16 + wgp) * 64 + ev * 8 + 2 * pm + (j & 1);
  }
  auto ks_of = [&](int cc) {
    return cc < 4 ? 4 * (8 + 4 * kq + 2 * (cc & 1) + (cc >> 1)) : cc < 6 ? 4 * (2 * kq + cc - 4) : cc == 6 ? 4 * (24 + kq) : 112 + 8 * kq + 4 * (cc - 7);
  };
  u32x4 af[9][4];
  auto issueW = [&](auto C_, auto J_) {
    constexpr int cc = decltype(C_)::value, j = decltype(J_)::value;
    if constexpr (cc < 9) {
      const int ks = ks_of(cc);
      const char* wb = cc < 7 ? wdb + (size_t)ks * 16 * PX : wob + (size_t)(ks - 112) * 16 * PX;
      dma16(L0 + (cc & 3) * SLOT2 + kq * 8192 + rt * 4096 + j * 1024, vo_w, wb + j * 8 * PX);
    }
  };
  auto issueA = [&](auto C_, auto J_) {
    constexpr int cc = decltype(C_)::value, j = decltype(J_)::value;
    af[cc][j] = load_sc1(rs_ws, a2c + (ks_of(cc) + j) * 1024);
  };
  auto pref = [&](auto I_) {
    constexpr int i = decltype(I_)::value;
    if constexpr (i < 24) {
      if constexpr (i & 1) issueA(IC<i / 8>{}, IC<(i % 8) / 2>{}); else issueW(IC<i / 8>{}, IC<(i % 8) / 2>{});
    } else if constexpr (i < 28) issueA(IC<3>{}, IC<i - 24>{});
  };
  wait_flags(fl + fw, lane < 8, tag);
  publish(3);
  lds_bar();
  STAMP(6);

  {
    const lds_ptr kp = L + KVB + m * 4096 + n * 2048 + lane * 16;
    const bf16x8 qf = *(LAS bf16x8*)(L + QB + m * 2048 + n * 1024 + lane * 16);
    const int hs = tp & 1;
    const unsigned va = KVB + m * 4096 + n * 2048 + 1024 + (hs * 32 + ((4 * h + tq) ^ (4 * hs))) * 16 + (tp >> 1) * 8;
    const int qq = r32 - 4 * h;
    auto kfr = [&](int t) { return *(LAS bf16x8*)(kp + t * 4096); };
    auto mask = [&](f32x16& s, bool diag) {
#pragma unroll
      for (int r = 0; r < 16; ++r) {
        const bool le = ((r & 3) + 8 * (r >> 2)) <= qq;
        s[r] = (le == diag) ? s[r] : -INFINITY;
      }
    };
    auto rmax = [&](const f32x16& s, float a) {
#pragma unroll
      for (int r = 0; r < 16; r += 2) a = __builtin_fmaxf(__builtin_fmaxf(a, s[r]), s[r + 1]);
      return a;
    };
    auto expo = [&](const f32x16& s, u32x4& p0, u32x4& p1) {
#pragma unroll
      for (int j = 0; j < 4; ++j) p0[j] = pk2(__builtin_amdgcn_exp2f(s[2 * j]), __builtin_amdgcn_exp2f(s[2 * j + 1]));
#pragma unroll
      for (int j = 0; j < 4; ++j) p1[j] = pk2(__builtin_amdgcn_exp2f(s[8 + 2 * j]), __builtin_amdgcn_exp2f(s[8 + 2 * j + 1]));
    };
    f32x16 nm, o = zero16;
    auto set_nm = [&](float mx) {
      const float mm = -__builtin_fmaxf(mx, swap32(mx));
#pragma unroll
      for (int r = 0; r < 16; ++r) nm[r] = mm;
    };
    if (ib >= 2) {
      bf16x8 kf[9];
#pragma unroll
      for (int t = 0; t < 9; ++t) kf[t] = kfr(t);
      f32x16 sb[2];
      float mx = -INFINITY;
      sb[0] = MFMA(kf[0], qf, zero16);
      static_for<0, 9>([&](auto T_) {
        constexpr int t = decltype(T_)::value;
        if constexpr (t + 1 < 9) sb[(t + 1) & 1] = MFMA(kf[t + 1], qf, zero16);
        pref(IC<t>{});
        SBAR();
        if constexpr (t == 0) mask(sb[0], false);
        if constexpr (t == 8) mask(sb[0], true);
        mx = rmax(sb[t & 1], mx);
        SBAR();
      });
      set_nm(mx);
      const unsigned vstep = blk ? 0u : 4096u;
      unsigned vb = blk ? (unsigned)ONES : va;
      bf16x8 v0 = tr_frag(L + vb, 128), v1 = tr_frag(L + vb + 256, 128);
      sb[0] = MFMA(kf[0], qf, nm);
      static_for<0, 9>([&](auto T_) {
        constexpr int t = decltype(T_)::value;
        if constexpr (t + 1 < 9) sb[(t + 1) & 1] = MFMA(kf[t + 1], qf, nm);
        static_for<0, 3>([&](auto I_) { pref(IC<9 + 3 * t + decltype(I_)::value>{}); });
        SBAR();
        if constexpr (t == 0) mask(sb[0], false);
        if constexpr (t == 8) mask(sb[0], true);
        u32x4 p0, p1;
        expo(sb[t & 1], p0, p1);
        o = MFMA(v0, __builtin_bit_cast(bf16x8, p0), o);
        o = MFMA(v1, __builtin_bit_cast(bf16x8, p1), o);
        if constexpr (t + 1 < 9) { vb += vstep; v0 = tr_frag(L + vb, 128); v1 = tr_frag(L + vb + 256, 128); }
        SBAR();
      });
    } else {
      static_for<0, 28>(pref);
      const int kt0 = 8 - (ib * 4 + m);
      float mx;
      { f32x16 s = MFMA(kfr(8), qf, zero16); mask(s, true); mx = rmax(s, -INFINITY); }
#pragma unroll 1
      for (int t = kt0; t < 8; ++t) { f32x16 s = MFMA(kfr(t), qf, zero16); mx = rmax(s, mx); }
      set_nm(mx);
      auto pv = [&](int t, f32x16& s) {
        u32x4 p0, p1;
        expo(s, p0, p1);
        const unsigned vb = blk ? (unsigned)ONES : va + t * 4096;
        const bf16x8 v0 = tr_frag(L + vb, 128), v1 = tr_frag(L + vb + 256, 128);
        o = MFMA(v0, __builtin_bit_cast(bf16x8, p0), o);
        o = MFMA(v1, __builtin_bit_cast(bf16x8, p1), o);
      };
      { f32x16 s = MFMA(kfr(8), qf, nm); mask(s, true); pv(8, s); }
#pragma unroll 1
      for (int t = kt0; t < 8; ++t) { f32x16 s = MFMA(kfr(t), qf, nm); pv(t, s); }
    }
    store_sc1(rs_ws, a2row + (112 + idx * 4 + tyB * 2 + n) * 1024, sc_frag(o, 0, __builtin_amdgcn_rcpf(o[8])));
  }
  STAMP(7);

  {
    unsigned aw[2];
#pragma unroll
    for (int t = 0; t < 2; ++t)
      aw[t] = kq * 8192 + (4 * h + tq) * 128 + (((4 * t + 2 * blk + (tp >> 1)) ^ (4 * ((tq >> 1) & 1))) * 16) + (tp & 1) * 8;
    acc[0] = zero16; acc[1] = zero16;
    auto rd = [&](auto G_) {
      constexpr int g = decltype(G_)::value;
      constexpr int base = ((g / 4) & 1) * SLOT2 + (g & 3) * 2048;
      wf[g & 1][0] = tr_frag(L + base + aw[0], 1024);
      wf[g & 1][1] = tr_frag(L + base + aw[1], 1024);
    };
    wait_flags(fl + fw, lane >= 8 && lane < 14, tag);
    publish(4);
    STAMP(8);
    const u32x4 xres = *(const u32x4*)(x + (size_t)(p * 64 + (tid >> 3)) * PX + c * 128 + (tid & 7) * 16);
    lds_bar();
    STAMP(9);
    rd(IC<0>{});
    static_for<0, 36>([&](auto G_) {
      constexpr int g = decltype(G_)::value;
      constexpr int cc = g / 4, j = g & 3;
      if constexpr (j == 3 && cc + 1 < 9) {
        constexpr int young = (cc + 2 < 9 ? 4 : 0) + (cc + 3 < 9 ? 3 : 0);
        if constexpr (cc >= 2) wait_bar<young + (cc == 2 ? 7 : cc == 3 ? 4 : cc == 5 ? 3 : cc == 6 ? 7 : 0)>(); else lds_bar();
        if constexpr (cc & 1) adv(aw, (cc & 2) ? -65536 : 65536);
      }
      if constexpr (g == 20) wait_flags(fl + fw, lane >= 14 && lane < 22, tag);
      if constexpr (g + 1 < 36) rd(IC<g + 1>{});
      SBAR();
      const bf16x8 a = __builtin_bit_cast(bf16x8, af[cc][j]);
      acc[0] = MFMA(wf[g & 1][0], a, acc[0]);
      issueW(IC<cc + 3>{}, IC<j>{});
      if constexpr (cc < 3) issueA(IC<cc + 4>{}, IC<j>{});
      if constexpr (cc == 5 || cc == 6) issueA(IC<cc + 2>{}, IC<j>{});
      acc[1] = MFMA(wf[g & 1][1], a, acc[1]);
      SBAR();
    });
    STAMP(10);
    lds_bar();
    constexpr int PP = 272;
#pragma unroll
    for (int t = 0; t < 2; ++t)
#pragma unroll
      for (int g4 = 0; g4 < 4; ++g4) {
        f32x4 v = {acc[t][4 * g4], acc[t][4 * g4 + 1], acc[t][4 * g4 + 2], acc[t][4 * g4 + 3]};
        *(LAS f32x4*)(L + kq * 64 * PP + (rt * 32 + r32) * PP + (t * 32 + 8 * g4 + 4 * h) * 4) = v;
      }
    lds_bar();
    const lds_ptr q = L + (tid >> 3) * PP + (tid & 7) * 32;
    f32x4 lo = *(LAS f32x4*)q, hi = *(LAS f32x4*)(q + 16);
#pragma unroll
    for (int k = 1; k < 4; ++k) { lo += *(LAS f32x4*)(q + k * 64 * PP); hi += *(LAS f32x4*)(q + k * 64 * PP + 16); }
    u32x4 o;
#pragma unroll
    for (int j = 0; j < 2; ++j) {
      o[j] = pk2(lo[2 * j] + __uint_as_float(xres[j] << 16), lo[2 * j + 1] + __uint_as_float(xres[j] & 0xffff0000u));
      o[2 + j] = pk2(hi[2 * j] + __uint_as_float(xres[2 + j] << 16), hi[2 * j + 1] + __uint_as_float(xres[2 + j] & 0xffff0000u));
    }
    *(u32x4*)(out + (size_t)(p * 64 + (tid >> 3)) * PX + c * 128 + (tid & 7) * 16) = o;
  }
  if (b == 0) {
#pragma unroll
    for (int k = 0; k < 4; ++k) wait_flags(fl + (size_t)(k * 64 + lane) * 64 + 24 + w, true, tag);
    lds_bar();
    if (tid == 0) __hip_atomic_store((GAS u64*)(ws + WS_EP), tag, RLX_AGENT);
  }
  STAMP(11);
  STAMP_OUT(0);
}

extern "C" void kernel_launch(void* const* d_in, const int* in_sizes, int n_in, void* d_out, int out_size,
                              void* d_ws, size_t ws_size, hipStream_t stream) {
  fused<<<256, 512, 0, stream>>>((const char*)d_in[0], (const char*)d_in[1], (const char*)d_in[2], (const char*)d_in[4],
                                 (const char*)d_in[5], (const char*)d_in[6], (char*)d_ws, (char*)d_out);
}
```

```cpp
#include <hip/hip_runtime.h>
#include <hip/hip_bf16.h>
#include <stdint.h>
#include <type_traits>

typedef unsigned long long u64;
typedef __attribute__((ext_vector_type(8))) __bf16 bf16x8;
typedef __attribute__((ext_vector_type(2))) __bf16 bf16x2;
typedef __attribute__((ext_vector_type(4))) short s16x4;
typedef __attribute__((ext_vector_type(16))) float f32x16;
typedef __attribute__((ext_vector_type(4))) float f32x4;
typedef __attribute__((ext_vector_type(4))) unsigned u32x4;
typedef __attribute__((ext_vector_type(2))) unsigned u32x2;
#define LAS __attribute__((address_space(3)))
#define GAS __attribute__((address_space(1)))
#define CAS __attribute__((address_space(4)))
#define DI __device__ __forceinline__
typedef LAS char* lds_ptr;
template <int V> using IC = std::integral_constant<int, V>;
#define CV(X) decltype(X)::value

#define MFMA(a, b, c) __builtin_amdgcn_mfma_f32_32x32x16_bf16((a), (b), (c), 0, 0, 0)
#define MFMA16(a, b, c) __builtin_amdgcn_mfma_f32_16x16x32_bf16((a), (b), (c), 0, 0, 0)
#define RLX_AGENT __ATOMIC_RELAXED, __HIP_MEMORY_SCOPE_AGENT
#define SBAR() __builtin_amdgcn_sched_barrier(0)

constexpr int PX = 1024;
constexpr int PQ = 3072;
constexpr int PF = 3584;
#define SAUX 16
#define FM 0
constexpr int FR = FM ? 2048 : 1024;
constexpr size_t WS_A2 = 0;
constexpr size_t WS_KV = 9216 * FR;
constexpr size_t WS_Q  = WS_KV + 4096 * FR;
constexpr size_t WS_CS = WS_Q + 1024 * FR;
constexpr size_t WS_RS = WS_CS + 393216;
constexpr size_t WS_FL = WS_RS + 32768;
constexpr size_t WS_EP = WS_FL + 131072;
constexpr size_t WS_ST = WS_EP + 4096;
constexpr int S32 = 32768;
constexpr int KVB = 98304, OWN = 131072, CSB = 147456, RSB = 155648, POLB = 157696, ONES = 159744, CNT = 160256, DUM = 160768;

template <int I, int N, class F>
DI void static_for(F&& f) {
  if constexpr (I < N) { f(IC<I>{}); static_for<I + 1, N>(f); }
}
DI u32x4 mk_rs(const void* p, unsigned bytes) {
  const u64 a = (u64)p;
  return (u32x4){(unsigned)a, (unsigned)(a >> 32), bytes, 0x00020000u};
}
template <int LI, int GI>
DI void dma(unsigned lb, unsigned go, unsigned vo, u32x4 rs) {
  unsigned t;
  asm volatile("s_add_u32 m0, %1, %5\n\ts_add_u32 %0, %2, %6\n\tbuffer_load_dwordx4 %3, %4, %0 offen lds"
               : "=&s"(t) : "s"(lb), "s"(go), "v"(vo), "s"(rs), "n"(LI), "n"(GI) : "memory", "scc");
}
template <int LI>
DI void dma_s(unsigned lb, unsigned go, unsigned gi, unsigned vo, u32x4 rs) {
  unsigned t;
  asm volatile("s_add_u32 m0, %1, %5\n\ts_add_u32 %0, %2, %6\n\tbuffer_load_dwordx4 %3, %4, %0 offen lds"
               : "=&s"(t) : "s"(lb), "s"(go), "v"(vo), "s"(rs), "n"(LI), "s"(gi) : "memory", "scc");
}
template <int LI, int GI>
DI void dma_sc1(unsigned lb, unsigned go, unsigned vo, u32x4 rs) {
  unsigned t;
  asm volatile("s_add_u32 m0, %1, %5\n\ts_add_u32 %0, %2, %6\n\tbuffer_load_dwordx4 %3, %4, %0 offen sc1 lds"
               : "=&s"(t) : "s"(lb), "s"(go), "v"(vo), "s"(rs), "n"(LI), "n"(GI) : "memory", "scc");
}
template <int LI, int GI>
DI void dma4(unsigned go, unsigned vo, u32x4 rs) {
  unsigned t;
  asm volatile("s_mov_b32 m0, %4\n\ts_add_u32 %0, %1, %5\n\tbuffer_load_dword %2, %3, %0 offen lds"
               : "=&s"(t) : "s"(go), "v"(vo), "s"(rs), "n"(LI), "n"(GI) : "memory", "scc");
}
template <int LI, int GI>
DI void dma_sc1_mask(unsigned mask, unsigned lb, unsigned go, unsigned vo, u32x4 rs) {
  unsigned t;
  asm volatile("s_add_u32 m0, %1, %5\n\ts_add_u32 %0, %2, %6\n\ts_mov_b32 exec_lo, %7\n\ts_mov_b32 exec_hi, 0\n\t"
               "buffer_load_dwordx4 %3, %4, %0 offen sc1 lds\n\ts_mov_b64 exec, -1"
               : "=&s"(t) : "s"(lb), "s"(go), "v"(vo), "s"(rs), "n"(LI), "n"(GI), "s"(mask) : "memory", "scc");
}
DI void flag_store(u64 v, unsigned so, u32x4 rs) {
  asm volatile("s_mov_b64 exec, 1\n\tbuffer_store_dwordx2 %0, off, %2, %1 sc1\n\ts_mov_b64 exec, -1" ::"v"(v), "s"(so), "s"(rs) : "memory");
}
template <int GI>
DI void ld_sc1(u32x4& d, unsigned go, unsigned vo, u32x4 rs) {
  unsigned t;
  asm volatile("s_add_u32 %1, %2, %5\n\tbuffer_load_dwordx4 %0, %3, %4, %1 offen sc1"
               : "=v"(d), "=&s"(t) : "s"(go), "v"(vo), "s"(rs), "n"(GI) : "memory", "scc");
}
template <int GI>
DI void ld8(u32x2& d, unsigned go, unsigned vo, u32x4 rs) {
  unsigned t;
  asm volatile("s_add_u32 %1, %2, %5\n\tbuffer_load_dwordx2 %0, %3, %4, %1 offen"
               : "=v"(d), "=&s"(t) : "s"(go), "v"(vo), "s"(rs), "n"(GI) : "memory", "scc");
}
DI void landed(u32x2& a, u32x2& b) { asm volatile("" : "+v"(a), "+v"(b) :: "memory"); }
template <int N> DI void land1(u32x4& a) { asm volatile("s_waitcnt vmcnt(%1)" : "+v"(a) : "n"(N) : "memory"); }
template <int N> DI void land2(u32x4& a, u32x4& b) { asm volatile("s_waitcnt vmcnt(%2)" : "+v"(a), "+v"(b) : "n"(N) : "memory"); }
#define TS(ID)
#define WB(N, ID) wait_bar<N>()
template <int N>
DI void wait_bar() { asm volatile("s_waitcnt vmcnt(%0) lgkmcnt(0)\n\ts_barrier" ::"n"(N) : "memory"); }
template <int N>
DI void wait_vm() { asm volatile("s_waitcnt vmcnt(%0)" ::"n"(N) : "memory"); }
DI void lds_bar() { asm volatile("s_waitcnt lgkmcnt(0)\n\ts_barrier" ::: "memory"); }
DI void wait_lds() { asm volatile("s_waitcnt lgkmcnt(0)" ::: "memory"); }
DI void pinu(unsigned& x) { asm volatile("" : "+v"(x)); }
DI void pinf(float& x) { asm volatile("" : "+v"(x)); }
DI void pinb(bf16x8& x) { asm volatile("" : "+v"(x)); }
DI void pinq(f32x4& x) { asm volatile("" : "+v"(x)); }
DI void pins(unsigned& x) { asm volatile("" : "+s"(x)); }
DI int lane_again() {
  unsigned z;
  asm volatile("s_mov_b32 %0, -1" : "=s"(z));
  return __builtin_amdgcn_mbcnt_hi(z, __builtin_amdgcn_mbcnt_lo(z, 0u));
}
DI bf16x8 tr_frag(lds_ptr p, int off1) {
  s16x4 a = __builtin_amdgcn_ds_read_tr16_b64_v4i16((LAS s16x4*)p);
  s16x4 b = __builtin_amdgcn_ds_read_tr16_b64_v4i16((LAS s16x4*)(p + off1));
  return __builtin_bit_cast(bf16x8, __builtin_shufflevector(a, b, 0, 1, 2, 3, 4, 5, 6, 7));
}
DI unsigned pk2(float a, float b) { bf16x2 v = {(__bf16)a, (__bf16)b}; return __builtin_bit_cast(unsigned, v); }
struct GeluT { float t0, t1, w0, w1; };
template <int K>
DI void gelu5(GeluT& s, unsigned& r, float a0, float a1, float b0, float b1, float C1, float C2, float V) {
  if constexpr (K == 0)
    asm volatile("v_mul_f32 %0, %2, %2\n\tv_mul_f32 %1, %3, %3\n\tv_fma_f32 %0, %0, %5, %4\n\tv_fma_f32 %1, %1, %5, %4"
                 : "=&v"(s.t0), "=&v"(s.t1) : "v"(a0), "v"(a1), "v"(C1), "v"(C2));
  if constexpr (K == 1)
    asm volatile("v_mul_f32 %0, %4, %0\n\tv_mul_f32 %1, %5, %1\n\tv_mul_f32 %2, %4, %6\n\tv_mul_f32 %3, %5, %7"
                 : "+v"(s.t0), "+v"(s.t1), "=&v"(s.w0), "=&v"(s.w1) : "v"(a0), "v"(a1), "v"(b0), "v"(b1));
  if constexpr (K == 2) asm volatile("v_exp_f32 %0, %0\n\tv_exp_f32 %1, %1" : "+v"(s.t0), "+v"(s.t1));
  if constexpr (K == 3)
    asm volatile("v_fma_f32 %0, %0, %2, %2\n\tv_fma_f32 %1, %1, %2, %2\n\tv_rcp_f32 %0, %0" : "+v"(s.t0), "+v"(s.t1) : "v"(V));
  if constexpr (K == 4)
    asm volatile("v_rcp_f32 %2, %2\n\tv_mul_f32 %3, %3, %1\n\tv_mul_f32 %4, %4, %2\n\tv_cvt_pk_bf16_f32 %0, %3, %4"
                 : "=v"(r), "+v"(s.t0), "+v"(s.t1), "+v"(s.w0), "+v"(s.w1));
}
template <int K>
DI void gelu4(GeluT& s, unsigned& r, float a0, float a1, float b0, float b1, float C1, float C2, float V) {
  if constexpr (K == 0)
    asm volatile("v_mul_f32 %0, %2, %2\n\tv_mul_f32 %1, %3, %3\n\tv_fma_f32 %0, %0, %5, %4\n\tv_fma_f32 %1, %1, %5, %4\n\tv_mul_f32 %0, %2, %0"
                 : "=&v"(s.t0), "=&v"(s.t1) : "v"(a0), "v"(a1), "v"(C1), "v"(C2));
  if constexpr (K == 1)
    asm volatile("v_mul_f32 %1, %2, %1\n\tv_exp_f32 %0, %0\n\tv_exp_f32 %1, %1" : "+v"(s.t0), "+v"(s.t1) : "v"(a1));
  if constexpr (K == 2)
    asm volatile("v_mul_f32 %2, %4, %6\n\tv_fma_f32 %0, %0, %8, %8\n\tv_fma_f32 %1, %1, %8, %8\n\tv_rcp_f32 %0, %0\n\tv_mul_f32 %3, %5, %7"
                 : "+v"(s.t0), "+v"(s.t1), "=&v"(s.w0), "=&v"(s.w1) : "v"(a0), "v"(a1), "v"(b0), "v"(b1), "v"(V));
  if constexpr (K == 3) gelu5<4>(s, r, a0, a1, b0, b1, C1, C2, V);
}
DI unsigned scale2(float a0, float a1, float s) {
  unsigned r; float t;
  asm volatile("v_mul_f32 %0, %2, %4\n\tv_mul_f32 %1, %3, %4\n\tv_cvt_pk_bf16_f32 %0, %0, %1" : "=&v"(r), "=&v"(t) : "v"(a0), "v"(a1), "v"(s));
  return r;
}
DI void exp_pk2(unsigned& r0, unsigned& r1, float a, float b, float c, float d) {
  float t0, t1;
  asm volatile("v_exp_f32 %0, %4\n\tv_exp_f32 %2, %5\n\tv_exp_f32 %1, %6\n\tv_exp_f32 %3, %7\n\tv_cvt_pk_bf16_f32 %0, %0, %2\n\tv_cvt_pk_bf16_f32 %1, %1, %3"
               : "=&v"(r0), "=&v"(r1), "=&v"(t0), "=&v"(t1) : "v"(a), "v"(b), "v"(c), "v"(d));
}
constexpr u64 tri_mask(int r) {
  u64 mk = 0;
  for (int l = 0; l < 64; ++l)
    if ((r & 3) + 8 * (r >> 2) + 4 * (l >> 5) <= (l & 31)) mk |= 1ull << l;
  return mk;
}
DI void exp_tri(unsigned& pd, unsigned& pf, float d0, float d1, float f0, float f1, u64 m0_, u64 m1_) {
  float t0, t1;
  asm volatile("v_cndmask_b32 %2, %6, %4, %8\n\tv_cndmask_b32 %3, %7, %5, %9\n\tv_exp_f32 %2, %2\n\tv_exp_f32 %3, %3\n\t"
               "v_cndmask_b32 %0, 0, %2, %8\n\tv_cvt_pk_bf16_f32 %1, %2, %3\n\tv_cndmask_b32 %3, 0, %3, %9\n\t"
               "v_cvt_pk_bf16_f32 %0, %0, %3\n\tv_xor_b32 %1, %1, %0"
               : "=&v"(pd), "=&v"(pf), "=&v"(t0), "=&v"(t1) : "v"(d0), "v"(d1), "v"(f0), "v"(f1), "s"(m0_), "s"(m1_));
}
DI float swap32(float v) {
  auto r = __builtin_amdgcn_permlane32_swap(__float_as_uint(v), __float_as_uint(v), false, false);
  return (threadIdx.x & 32) ? __uint_as_float(r[0]) : __uint_as_float(r[1]);
}
template <int CTRL, int RM>
DI float dpp_add(float v) {
  return v + __int_as_float(__builtin_amdgcn_update_dpp(0, __float_as_int(v), CTRL, RM, 0xf, false));
}
DI float wave_sum(float v) {
  v = dpp_add<0x111, 0xf>(v); v = dpp_add<0x112, 0xf>(v); v = dpp_add<0x114, 0xf>(v); v = dpp_add<0x118, 0xf>(v);
  v = dpp_add<0x142, 0xa>(v); v = dpp_add<0x143, 0xc>(v);
  return __int_as_float(__builtin_amdgcn_readlane(__float_as_int(v), 63));
}
DI void split3(float c, unsigned& t1, unsigned& t2, unsigned& t3) {
  t1 = __float_as_uint(c);
  const float r = c - __uint_as_float(t1 & 0xffff0000u);
  t2 = __float_as_uint(r);
  t3 = __float_as_uint(r - __uint_as_float(t2 & 0xffff0000u));
}
DI float sum4(unsigned a, unsigned b, unsigned c, unsigned d) {
  float r, t;
  asm volatile("v_add_f32 %0, %2, %3\n\tv_add_f32 %1, %4, %5\n\tv_add_f32 %0, %0, %1" : "=&v"(r), "=&v"(t) : "v"(a), "v"(b), "v"(c), "v"(d));
  return r;
}
#define BALLOT(x) __builtin_amdgcn_ballot_w64(x)
#define RARE(x) __builtin_expect(!!(x), 0)
DI unsigned hi2(unsigned lo, unsigned hi) { return __builtin_amdgcn_perm(hi, lo, 0x07060302u); }
DI void store_sc1(__amdgpu_buffer_rsrc_t rs, unsigned off, u32x4 v) {
  __builtin_amdgcn_raw_buffer_store_b128(v, rs, off, 0, 16);
}
DI unsigned spread(unsigned o) {
  unsigned r;
  asm volatile("v_and_b32 %0, %2, %1\n\tv_add_u32 %0, %0, %1" : "=&v"(r) : "v"(o), "n"(FM));
  return r;
}
DI void store_fr(__amdgpu_buffer_rsrc_t rs, unsigned so, unsigned o, u32x4 v) {
  unsigned r;
  asm volatile("v_and_b32 %0, %2, %1\n\tv_add3_u32 %0, %0, %1, %3" : "=&v"(r) : "v"(o), "n"(FM), "s"(so));
  __builtin_amdgcn_raw_buffer_store_b128(v, rs, r, 0, SAUX);
}
DI u32x4 load_sc1(__amdgpu_buffer_rsrc_t rs, unsigned off) {
  return __builtin_bit_cast(u32x4, __builtin_amdgcn_raw_buffer_load_b128(rs, off, 0, 16));
}

#define STAMP(k)
#define STAMP_DECL
#define STAMP_OUT(kid)

enum { K_STG, K_W2, K_F, K_KV, K_Q, K_CS, K_RS, K_POLL, K_EXT, K_ROW, K_TW, K_TP, K_XR };
enum { X_T, X_H0, X_H1, X_H2, X_H3, X_H4, X_QKV, X_PUB, X_O };
struct Ev { int pos, kind, a, b; };
struct Tab {
  Ev e[200]; int n;
  constexpr Tab() : e{}, n(0) {}
  constexpr void add(int pos, int kind, int a = 0, int b = 0) { e[n].pos = pos; e[n].kind = kind; e[n].a = a; e[n].b = b; ++n; }
  constexpr int find(int pos, int k) const {
    for (int i = 0; i < n; ++i) if (e[i].pos == pos && k-- == 0) return i;
    return -1;
  }
  constexpr int younger(int kind, int a, int at, int b = -1) const {
    int p = -1000, c = 0;
    for (int i = 0; i < n; ++i) if (e[i].kind == kind && e[i].a == a && (b < 0 || e[i].b == b) && e[i].pos > p) p = e[i].pos;
    for (int i = 0; i < n; ++i) if (e[i].pos > p && e[i].pos < at
      ) ++c;
    return p == -1000 || p >= at ? -1 : (c > 63 ? 63 : c);
  }
};
constexpr int imin(int a, int b) { return a < b ? a : b; }
constexpr int PA(int g, int k) { return 96 + 2 * g + k; }
constexpr int PT(int i) { return 160 + i; }
#define CMIX 2
#define TWA {TP0, 76, 88, PA(4, 0), PA(8, 0), PA(21, 0), PA(26, 0), TP0 + 12, PA(23, 0)}
#define TWB {TP1, 68, 74, 82, 88, 100, 116, TP1 + 12, 128 + 10}
#define TWM 0x81
#define YNG (w >= 4)
#define XK 2
#define XV 4
#define TP0 9
#define TP1 9
constexpr Tab tabA() {
  Tab t;
  t.add(-100, K_ROW);
  for (int s = 0; s < 2; ++s) for (int j = 0; j < 5; ++j) t.add(-90 + 10 * s + j, K_STG, s, j);
  t.add(-70, K_EXT, X_T);
  for (int j = 0; j < 5; ++j) t.add(j, K_STG, 2, j);
  for (int s = 3; s < 10; ++s) {
    const int b0 = 12 * (s - 3) + 6;
    const int off[5] = {0, 2, 4, 7, 10};
    for (int j = 0; j < (s < 8 ? 5 : 4); ++j) t.add(b0 + off[j], K_STG, s, j);
  }
  t.add(45, K_RS); t.add(55, K_CS, 0);
  t.add(TP0 + 2, K_TP);
  { const int tw[9] = TWA; for (int i = 0; i < 9; ++i) if (TWM >> i & 1) t.add(tw[i], K_TW, i); }
  t.add(90, K_STG, 10, 0); t.add(92, K_STG, 10, 1); t.add(94, K_STG, 10, 2); t.add(PA(0, 0), K_STG, 10, 3);
  for (int f = 0; f < 5; ++f) t.add(PA(2 * f + 1, 1), K_EXT, X_QKV);
  for (int j = 0; j < 4; ++j) t.add(PA(10 + j, 0), K_STG, 11, j);
  t.add(PA(16, 1), K_EXT, X_PUB);
  for (int i = 0; i < 7; ++i) t.add(PA(14 + i, 0), K_W2, i);
  t.add(PA(24, 0), K_W2, 7);
  t.add(PA(22, 0), K_CS, 1);
  t.add(PA(24, 1), K_POLL, 0);
  for (int i = 0; i < 6; ++i) t.add(PA(30, 0) + i, K_KV, ((i >> 1) + 2) % 3, i & 1);
  t.add(PT(15), K_EXT, X_H3);
  t.add(PT(17), K_POLL, 1);
  t.add(PT(31), K_EXT, X_H4);
  return t;
}
constexpr Tab tabB() {
  Tab t;
  t.add(-100, K_ROW);
  for (int s = 0; s < 2; ++s) for (int j = 0; j < 4; ++j) t.add(-90 + 10 * s + j, K_STG, s, j);
  t.add(-76, K_STG, 2, 0);
  t.add(-70, K_EXT, X_T);
  t.add(0, K_STG, 2, 1); t.add(0, K_STG, 2, 2); t.add(1, K_STG, 2, 3);
  t.add(2, K_STG, 3, 0); t.add(3, K_STG, 3, 1); t.add(5, K_STG, 3, 2); t.add(7, K_STG, 3, 3);
  for (int s = 4; s < 12; ++s) for (int j = 0; j < 4; ++j) t.add(8 * (s - 4) + 4 + 2 * j, K_STG, s, j);
  t.add(29, K_RS); t.add(45, K_CS, 0);
  t.add(TP1 + 2, K_TP);
  { const int tw[9] = TWB; for (int i = 0; i < 9; ++i) if (TWM >> i & 1) t.add(tw[i], K_TW, i); }
  for (int j = 0; j < 4; ++j) t.add(2 * (39 + 3 * j), K_STG, 12, j);
  t.add(91, K_EXT, X_H0);
  t.add(2 * 47, K_CS, 1);
  t.add(111, K_EXT, X_H1);
  for (int j = 0; j < 4; ++j) t.add(2 * (51 + 3 * j), K_STG, 13, j);
  t.add(2 * 59, K_W2, 0); t.add(2 * 62, K_W2, 1); t.add(2 * 63, K_W2, 2); t.add(128, K_W2, 3);
  t.add(127, K_EXT, X_PUB);
  for (int i = 4; i < 8; ++i) t.add(128 + 2 * i - 6, K_W2, i);
  t.add(128 + 12, K_CS, 2);
  t.add(128 + 15, K_POLL, 0);
  t.add(128 + 20, K_EXT, X_H2);
  for (int i = 0; i < 6; ++i) t.add(128 + 25 + i, K_KV, i >> 1, i & 1);
  t.add(128 + 31, K_Q);
  t.add(PT(8), K_EXT, X_H3);
  t.add(PT(13), K_POLL, 1);
  t.add(PT(24), K_EXT, X_H4);
  return t;
}
#define PY 22
constexpr int FY(int i) { return 48 + PY + 14 + i; }
constexpr Tab tabC(int y) {
  Tab t;
  for (int i = 0; i < 6; ++i) t.add(i, K_W2, 8 + i);
  for (int i = 0; i < 8; ++i) t.add(6 + i, K_F, i >> 1, i & 1);
  for (int i = 0; i < 6; ++i) t.add(14 + i, K_W2, 14 + i);
  for (int i = 0; i < 6; ++i) t.add(48 + 1 + i, K_F, 4 + (i >> 1), i & 1);
  t.add(48 + 8, K_F, 7, 0); t.add(48 + 9, K_F, 7, 1);
  for (int i = 20; i < 24; ++i) t.add(48 + i - 10, K_W2, i);
  for (int i = 0; i < 8; ++i) t.add(48 + 14 + i, K_F, 8 + (i >> 1), i & 1);
  for (int i = 24; i < 28; ++i) t.add(48 + i - 2, K_W2, i);
  for (int i = 0; i < 4; ++i) t.add(48 + 26 + i, K_F, 12 + (i >> 1), i & 1);
  if (y == 0) for (int i = 0; i < 4; ++i) t.add(48 + 30 + (i >> 1) * 10, K_XR, i);
  for (int i = 28; i < 32; ++i) t.add(48 + i + 3, K_W2, i);
  if (y < 2) t.add(48 + (y ? PY : 35), K_POLL, 4);
  for (int i = 32; i < 36; ++i) t.add(48 + i + 4, K_W2, i);
  if (y < 2) for (int i = 0; i < 8; ++i) t.add(y ? FY(i) : 48 + 44 + i, K_F, 14 + (i & 3), 1 - (i >> 2));
  return t;
}
constexpr Tab TA = tabA(), TB = tabB(), TO = tabC(0), TY = tabC(1), TC = tabC(2);
constexpr const Tab& tab_of(int r) { return r == 0 ? TA : r == 1 ? TB : TC; }
constexpr int ycom(int kind, int a, int at) { return imin(TO.younger(kind, a, at), TY.younger(kind, a, at)); }
constexpr int fy_at(int pos) { for (int i = 0; i < 8; ++i) if (FY(i) == pos) return i; return -1; }

__global__ __launch_bounds__(512, 2) void fused(const char* x, const char* wqkv,
                                                const char* __restrict__ wo, const char* wg,
                                                const char* wu, const char* __restrict__ wd,
                                                char* ws, char* __restrict__ out) {
  __shared__ __attribute__((aligned(1024))) char smem[163840];
  STAMP_DECL
  STAMP(0);
  {
    u64 pc; unsigned len;
    asm volatile(".Lk_begin:\n\ts_getpc_b64 %0\n\ts_mov_b32 %1, .Lk_end-.Lk_begin" : "=s"(pc), "=s"(len));
    dma4<DUM, 0>((blockIdx.x >> 3) * 2048u, threadIdx.x * 4u, mk_rs((const void*)pc, len));
  }
  const lds_ptr L = (lds_ptr)smem;
  const unsigned L0 = (unsigned)(size_t)L;
  const int tid = threadIdx.x, lane = tid & 63;
  const int w = __builtin_amdgcn_readfirstlane(tid >> 6);
  const int m = w >> 1, n = w & 1;
  const int b = blockIdx.x, xcd = b & 7, rr = b >> 3;
  const int ib = (xcd >> 2) * 8 + (rr >> 2);
  const int idx = 2 * (xcd & 3) + (rr & 1);
  const int tyB = (rr >> 1) & 1;
  const int rot = ib + rr;
  const bool job = w == ((ib >> 2) & 1) + 2 * (idx & 1);
  auto kc = [&](int c) { return (unsigned)(((c ^ (ib >> 1)) & 4) + ((c + rot) & 3)); };
  auto qcol = [&](int t) { return (unsigned)((idx ^ (t == 1 ? XK : t == 2 ? XV : 0)) * 128); };
  const unsigned lb1 = L0 + w * 1024;
  const int mat = w >> 2;
  unsigned vo_x, vo_p, vo_q;
  auto set_vo = [&](int lz) {
    const unsigned sw_w = (lz & 7) ^ (4 * ((lz >> 4) & 1));
    vo_x = (ib * 128 + 8 * w + (lz >> 3)) * PX + (((lz & 7) ^ ((4 * (w & 1) + (lz >> 4)) & 7)) * 16);
    vo_p = (16 * (w & 3) + (lz >> 3)) * PF + (sw_w ^ (2 * mat)) * 16;
    vo_q = (8 * w + (lz >> 3)) * PQ + sw_w * 16;
  };
  set_vo(lane);
  const unsigned lbp = L0 + mat * 16384 + (w & 3) * 2048;
  const int P0 = tyB ? 8 + 2 * idx : idx;
  asm volatile("" : "+s"(x), "+s"(wqkv), "+s"(wg), "+s"(wu), "+s"(ws)
               : "v"(vo_x), "v"(vo_p), "v"(vo_q), "v"(lane * 16), "s"(lb1), "s"(lbp), "s"(kc(0) * 128), "s"(kc(0) * (64 * PQ)), "s"(qcol(0)), "s"(qcol(1) + 1024), "s"(qcol(2) + 2048),
                 "s"(kc(0) * (64 * PF) + P0 * 128), "s"((unsigned)(ib * 128 + (tyB * 8 + idx) * 8 + w) * PX), "s"(tyB), "s"(mat));
  TS(1);
  const u32x4 rs_x = mk_rs(x, 2097152);
  const u32x4 rs_p = mk_rs(mat ? wu : wg, 512 * PF);
  const u32x4 rs_q = mk_rs(wqkv, 512 * PQ);
  constexpr int RING = 40960;
  auto slotB = [](int s) { return (s & 3) == 0 ? 3 : (s & 3) == 1 ? 2 : (s & 3) - 2; };
  auto issue_pair = [&](auto SL_, auto DP_, auto Q4_, auto J_) {
    constexpr int j = CV(J_);
    dma<CV(SL_) * S32 + (j >> 1) * 8192 + (j & 1) * 1024, CV(DP_) * 128 + (j & 1) * 8 * PF>(lbp, kc(2 * CV(Q4_) + (j >> 1)) * (64 * PF) + P0 * 128, vo_p, rs_p);
  };
  auto issueA = [&](auto S_, auto J_) {
    constexpr int s = CV(S_), j = CV(J_);
    if constexpr (s < 8) {
      constexpr int rg = (s + 1) % 3, xo = rg * RING + (rg == 0 ? 24576 : 0), wo_ = rg * RING + (rg == 0 ? 0 : 16384);
      if constexpr (j < 2) dma<xo + j * 8192, j * 64 * PX>(lb1, kc(s) * 128, vo_x, rs_x);
      else dma_s<wo_ + (j - 2) * 8192>(lb1, kc(s) * (64 * PQ), qcol(j - 2) + (j - 2) * 1024, vo_q, rs_q);
    } else {
      issue_pair(IC<(s == 8 ? 0 : s == 9 ? 1 : s == 10 ? 3 : 2)>{}, IC<0>{}, IC<s - 8>{}, J_);
    }
  };
  auto issueB = [&](auto S_, auto J_) {
    constexpr int s = CV(S_), j = CV(J_), sl = slotB(s) * S32;
    if constexpr (s < 8) {
      if constexpr (j < 2) dma<sl + 8192 + j * 16384, j * 64 * PX>(lb1, kc(s) * 128, vo_x, rs_x);
      else dma<sl + (j - 2) * 1024, (j - 2) * 8 * PF>(lbp, kc(s) * (64 * PF) + P0 * 128, vo_p, rs_p);
    } else if constexpr (s < 12) {
      issue_pair(IC<slotB(s)>{}, IC<1>{}, IC<s - 8>{}, J_);
    } else {
      dma<sl + j * 4096, 24 * 128>(L0 + mat * 16384 + (w & 3) * 1024, kc(4 * (s - 12) + j) * (64 * PF) + idx * 64, vo_p, rs_p);
    }
  };
  dma<CSB, 0>(lb1, (unsigned)(ib * 128 + (tyB * 8 + idx) * 8 + w) * PX, lane * 16, rs_x);
  TS(2);
  if (tyB) static_for<0, 4>([&](auto J_) { issueB(IC<0>{}, J_); });
  else     static_for<0, 5>([&](auto J_) { issueA(IC<0>{}, J_); });
  const u64 epoch = *(const CAS u64*)(ws + WS_EP);
  unsigned tlo, thi;
  const __amdgpu_buffer_rsrc_t rs_ws = __builtin_amdgcn_make_buffer_rsrc(ws, 0, (int)WS_EP, 0x00020000);
  const u32x4 rs_w4 = mk_rs(ws, (unsigned)WS_EP);
  const unsigned myfl = (unsigned)WS_FL + ((ib * 16 + tyB * 8 + idx) * 64 + w) * 8;
  auto publish = [&](int ev) { flag_store((u64)thi << 32 | tlo, myfl + 64 * ev, rs_w4); };
  *(LAS unsigned*)(L + ONES + (tid & 255) * 4) = (tid & 128) ? 0u : 0x3f803f80u;
  unsigned vo_l = lane * 16;
  pinu(vo_l);
  auto stats = [&](auto N_, auto&& op) {
    TS(3);
    wait_vm<CV(N_)>();
    TS(9);
    const u32x4 u = *(LAS u32x4*)(L + CSB + w * 1024 + vo_l);
    op(IC<0>{});
    float v[8];
#pragma unroll
    for (int j = 0; j < 4; ++j) { v[2 * j] = __uint_as_float(u[j] << 16); v[2 * j + 1] = __uint_as_float(u[j] & 0xffff0000u); }
    float a = ((v[0] + v[1]) + (v[2] + v[3])) + ((v[4] + v[5]) + (v[6] + v[7]));
    pinf(a); op(IC<1>{});
    float mean = wave_sum(a) * (1.f / 512.f);
    pinf(mean); op(IC<2>{});
    float q0 = 0.f, q1 = 0.f;
#pragma unroll
    for (int j = 0; j < 4; ++j) { q0 = __builtin_fmaf(v[j] - mean, v[j] - mean, q0); q1 = __builtin_fmaf(v[4 + j] - mean, v[4 + j] - mean, q1); }
    a = q0 + q1;
    pinf(a); op(IC<3>{});
    float V = wave_sum(a) * (1.f / 512.f) + 1e-5f;
    pinf(V); op(IC<4>{});
    const u64 e = epoch + 1;
    tlo = (unsigned)e; thi = (unsigned)(e >> 32);
    pins(tlo); pins(thi);
    if (vo_l == 0) store_sc1(rs_ws, (unsigned)WS_RS + (ib * 128 + (tyB * 8 + idx) * 8 + w) * 16, (u32x4){__float_as_uint(-mean), __float_as_uint(V), tlo, thi});
  };

  const f32x16 zero16 = {0, 0, 0, 0, 0, 0, 0, 0, 0, 0, 0, 0, 0, 0, 0, 0};
  const f32x4 zero4 = {0, 0, 0, 0};
  bf16x8 xf[32];
  unsigned ax[4], ag[2];
  auto set_ax = [&](unsigned base) {
    const int lz = lane_again(), h = lz >> 5, r32 = lz & 31;
#pragma unroll
    for (int s = 0; s < 4; ++s) { ax[s] = base + r32 * 128 + (((2 * s + h) ^ ((r32 >> 1) & 7)) * 16); pinu(ax[s]); }
  };
  auto set_ag = [&](auto K_, unsigned base) {
    if constexpr (CV(K_) == 0) { ag[0] = (((vo_l & 704) << 1) | ((vo_l & 256) << 6)) + base; pinu(ag[0]); }
    else { ag[0] |= (((vo_l >> 3) & 32) | ((vo_l >> 1) & 88)) ^ (64 * n); pinu(ag[0]); ag[1] = ag[0] ^ 32; pinu(ag[1]); }
  };
  auto mk_sel = [&]() {
    const int lz = lane_again();
    const unsigned sw = (((lz >> 4) ^ (lz >> 2)) & 1) ? 0u : 0x3f803f80u;
    return __builtin_bit_cast(bf16x8, (u32x4){sw, sw, sw, sw});
  };
  const unsigned cs_g = (unsigned)WS_CS + (tyB * 8 + idx) * 24576;
  auto put_cs = [&](int pass, int chunk, float c0, float c1, unsigned c2) {
    const int lz = lane_again();
    if (lz < 32) store_sc1(rs_ws, cs_g + pass * 8192 + kc(chunk) * 1024 + n * 512 + lz * 16, (u32x4){__float_as_uint(c0), __float_as_uint(c1), c2, tlo});
  };
  bf16x8 jfa[3][3][4];
  f32x4 jca[3][3];
  auto jobp = [&](auto K_, auto NT_, unsigned a0, unsigned a1, unsigned a2, int stp, auto P_, int chunk) {
    constexpr int k = CV(K_), NT = CV(NT_), pass = CV(P_);
    bf16x8 (&jf)[3][4] = jfa[pass];
    f32x4 (&jc)[3] = jca[pass];
    if (job) {
      if constexpr (k == 0) {
#pragma unroll
        for (int s = 0; s < 4; ++s) {
          jf[0][s] = tr_frag(L + a0 + s * stp, stp / 4);
          if constexpr (NT >= 2) jf[1][s] = tr_frag(L + a1 + s * stp, stp / 4);
          if constexpr (NT >= 3) jf[2][s] = tr_frag(L + a2 + s * stp, stp / 4);
        }
      }
      if constexpr (k == 1) {
        const bf16x8 sel = mk_sel();
#pragma unroll
        for (int t = 0; t < 3; ++t) jc[t] = zero4;
#pragma unroll
        for (int s = 0; s < 4; ++s)
#pragma unroll
          for (int t = 0; t < NT; ++t) jc[t] = MFMA16(sel, jf[t][s], jc[t]);
#pragma unroll
        for (int t = 0; t < NT; ++t) pinq(jc[t]);
      }
      if constexpr (k == 2) put_cs(pass, chunk, jc[0][0], NT >= 2 ? jc[1][0] : 0.f, NT == 3 ? __float_as_uint(jc[2][0]) : thi);
    }
  };
  float csum[3];
  u32x4 ce[4], re;
  unsigned cso;
  auto get_cs = [&](auto K_, auto P_, auto NT_) {
    constexpr int k = CV(K_), NT = CV(NT_);
    auto good = [&](const u32x4 e) { return BALLOT(NT == 3 ? e[3] == tlo : ((u64)e[3] << 32 | e[2]) == ((u64)tlo << 32 | thi)); };
    if constexpr (k == 0) {
      cso = (vo_l >> 9) * 3584 + vo_l + (CSB + n * 512); pinu(cso);
#pragma unroll
      for (int i = 0; i < 4; ++i) ce[i] = *(LAS u32x4*)(L + cso + i * 1024);
    }
    if constexpr (k == 1) {
      if (RARE(~(good(ce[0]) & good(ce[1]) & good(ce[2]) & good(ce[3])))) {
#pragma unroll 1
        for (unsigned spin = 0; spin < (1u << 16); ++spin) {
#pragma unroll
          for (int i = 0; i < 4; ++i) ce[i] = load_sc1(rs_ws, cs_g + CV(P_) * 8192 - CSB + cso + i * 1024);
          if (!~(good(ce[0]) & good(ce[1]) & good(ce[2]) & good(ce[3]))) break;
          __builtin_amdgcn_s_sleep(2);
        }
      }
    }
    if constexpr (k >= 2) csum[k - 2] = sum4(ce[0][k - 2], ce[1][k - 2], ce[2][k - 2], ce[3][k - 2]);
  };
  unsigned c3[3];
  auto corr2 = [&](auto K_, float c, bf16x8& o) {
    if constexpr (CV(K_) == 0) { split3(c, c3[0], c3[1], c3[2]); pinu(c3[1]); pinu(c3[2]); }
    else { o = __builtin_bit_cast(bf16x8, (u32x4){hi2(c3[0], c3[0]), hi2(c3[1], c3[1]), hi2(c3[0], c3[2]), hi2(c3[1], c3[2])}); pinb(o); }
  };
  float rstd, gC1, gC2, gV;
  bf16x8 mB;
  auto get_rs = [&](auto K_) {
    constexpr int k = CV(K_);
    auto good = [&](const u32x4 e) { return BALLOT(((u64)e[3] << 32 | e[2]) == ((u64)thi << 32 | tlo)); };
    if constexpr (k == 0) re = *(LAS u32x4*)(L + RSB + m * 512 + (vo_l & 511));
    if constexpr (k == 1) {
      if (RARE(~good(re))) {
#pragma unroll 1
        for (unsigned spin = 0; spin < (1u << 16); ++spin) {
          re = load_sc1(rs_ws, (unsigned)WS_RS + ib * 2048 + m * 512 + (vo_l & 511));
          if (!~good(re)) break;
          __builtin_amdgcn_s_sleep(2);
        }
      }
    }
    if constexpr (k == 2) {
      gV = __uint_as_float(re[1]);
      rstd = __builtin_amdgcn_rsqf(gV);
      gC1 = rstd * (-2.0f * 1.4426950408889634f * 0.7978845608028654f);
      gC2 = gC1 * rstd * rstd * 0.044715f;
      pinf(gC1); pinf(gC2);
    }
    if constexpr (k == 3) { split3(__uint_as_float(re[0]), c3[0], c3[1], c3[2]); pinu(c3[1]); pinu(c3[2]); }
    if constexpr (k == 4) {
      mB = __builtin_bit_cast(bf16x8, (u32x4){hi2(c3[0], c3[1]), hi2(c3[0], c3[1]), hi2(c3[2], c3[0]), hi2(c3[2], c3[1])}); pinb(mB);
    }
  };
  const unsigned a2row = (unsigned)(WS_A2 + (size_t)(ib * 4 + m) * 144 * FR);
  bf16x8 wf[2][3];
  GeluT gt;
  u32x4 hq;
  auto gelu20 = [&](auto I_, const f32x16& a, int ks) {
    constexpr int i = CV(I_), j = i / 5;
    unsigned r;
    gelu5<i % 5>(gt, r, a[2 * j], a[2 * j + 1], a[8 + 2 * j], a[9 + 2 * j], gC1, gC2, gV);
    if constexpr (i % 5 == 4) hq[j] = r;
    if constexpr (i == 19) store_fr(rs_ws, a2row + ks * FR, vo_l, hq);
  };
  auto gelu16 = [&](auto I_, const f32x16& a, int ks) {
    constexpr int i = CV(I_), j = i / 4;
    unsigned r;
    gelu4<i % 4>(gt, r, a[2 * j], a[2 * j + 1], a[8 + 2 * j], a[9 + 2 * j], gC1, gC2, gV);
    if constexpr (i % 4 == 3) hq[j] = r;
    if constexpr (i == 15) store_fr(rs_ws, a2row + ks * FR, vo_l, hq);
  };

  const int p = xcd * 4 + (rr >> 3), c = (rr & 7) ^ CMIX;
  const unsigned kw = (w + c + 2 * p) & 7;
  unsigned vo_w, vo_f, fwo, aw[2];
  const unsigned lb4 = L0 + w * 4096;
  unsigned lbq = L0 + w * 256;
  pins(lbq);
    auto set_vw = [&]() { vo_w = (vo_l >> 7) * PX + (((vo_l & 112) ^ ((vo_l >> 2) & 64)) + c * 128); pinu(vo_w); };
  unsigned tmpa;
  auto set_fw = [&](auto K_) {
    constexpr int k = CV(K_);
    if constexpr (k == 0) { tmpa = 0xc4b3a9u >> ((vo_l >> 3) & 28); fwo = (vo_l & 16) + (((p >> 1) * 16 + kw) * 64 + 4 * (p & 1)) * 8; }
    if constexpr (k == 1) fwo += (tmpa & 8) * 512 + (tmpa & 7) * 64;
    if constexpr (k == 2) tmpa = (vo_l & 48) * 512 + ((vo_l & 64) ? (idx ^ XV) * 512 : (idx ^ XK) * 512);
    if constexpr (k == 3) { tmpa = (vo_l & 48) == 48 ? 16384 + idx * 512 : tmpa; tmpa = (tmpa + ((ib - 2) * 1024 + (w & 6)) * 8) & 131071; }
    if constexpr (k == 4) fwo = (vo_l & 128) ? tmpa : fwo;
    pinu(fwo); pinu(tmpa);
  };
  auto set_aw = [&](auto K_) {
    if constexpr (CV(K_) == 0) { aw[0] = ((vo_l & 512) | ((vo_l & 192) << 1)) + w * 4096; pinu(aw[0]); }
    else { aw[0] |= ((vo_l >> 3) & 32) | ((vo_l >> 1) & 88); pinu(aw[0]); aw[1] = aw[0] ^ 64; pinu(aw[1]); }
  };
  auto k0 = [](int j) { return j < 8 ? 32 + j : j < 12 ? j - 8 : j < 14 ? 96 + j - 12 : 112 + j - 14; };
  auto km = [](int j) { return j < 8 ? 8 : j < 12 ? 4 : j < 14 ? 2 : 4; };
  auto issueW = [&](auto I_) {
    constexpr int i = CV(I_), j = i >> 1;
    constexpr int li = ((j >> 1) % 3) * S32 + (j & 1) * 2048 + (i & 1) * 1024;
    if constexpr (j < 14) dma<li, (k0(j) * 16 + (i & 1) * 8) * PX>(lb4, kw * (km(j) * 16 * PX), vo_w, mk_rs(wd, 1792 * PX));
    else dma<li, ((k0(j) - 112) * 16 + (i & 1) * 8) * PX>(lb4, kw * (km(j) * 16 * PX), vo_w, mk_rs(wo, 512 * PX));
  };
  u32x4 af[18][2];
  auto issueF = [&](auto J_, auto R_) {
    constexpr int j = CV(J_), rt = CV(R_);
    ld_sc1<(int)WS_A2 + k0(j) * FR>(af[j][rt], (p * 2 + rt) * (144 * FR) + kw * (km(j) * FR), vo_f, rs_w4);
  };
  bf16x8 w2[18][2];
  auto rdW = [&](auto J_, auto T_) {
    constexpr int j = CV(J_), t = CV(T_), sl = (j >> 1) % 3;
    w2[j][t] = tr_frag(L + (sl == 2 ? aw[t] + 65536 : aw[t]) + (sl & 1) * S32 + (j & 1) * 2048, 1024);
  };
  f32x16 acc2[2][2] = {{zero16, zero16}, {zero16, zero16}};
  auto mm2 = [&](auto J_, auto I_) {
    constexpr int j = CV(J_), rt = CV(I_) >> 1, t = CV(I_) & 1;
    acc2[rt][t] = MFMA(w2[j][t], __builtin_bit_cast(bf16x8, af[j][rt]), acc2[rt][t]);
  };
  auto poll_issue = [&](unsigned mask) { dma_sc1_mask<POLB, (int)WS_FL>(mask, lbq, 0u, fwo, rs_w4); };
  auto missing = [&](const u32x4 v, unsigned need0, unsigned need1) {
    const u64 tg = (u64)thi << 32 | tlo;
    const unsigned b0 = (unsigned)BALLOT(((u64)v[1] << 32 | v[0]) == tg), b1 = (unsigned)BALLOT(((u64)v[3] << 32 | v[2]) == tg);
    return (~b0 & need0) | (~b1 & need1);
  };
  auto spin_flags = [&](unsigned need0, unsigned need1) {
    const unsigned need = need0 | need1;
    const unsigned o = (unsigned)WS_FL + (((need >> ((vo_l >> 4) & 15)) & 1) ? fwo : (unsigned)__builtin_amdgcn_readlane(fwo, __builtin_ctz(need)));
#pragma unroll 1
    for (unsigned spin = 0; spin < (1u << 16); ++spin) {
      if (!missing(load_sc1(rs_ws, o), need0, need1)) break;
      __builtin_amdgcn_s_sleep(2);
    }
  };
  u32x4 pv;
  auto poll_read = [&]() { pv = *(LAS u32x4*)(L + POLB + w * 256 + (vo_l & 255)); };
  auto poll_check = [&](unsigned need0, unsigned need1) { if (RARE(missing(pv, need0, need1))) spin_flags(need0, need1); };
  const unsigned kvm = (ib >= 2 ? 0x7700u : ib == 1 ? 0x6600u : 0x4400u) | (tyB ? 0x800u : 0u);
  auto issueKV = [&](auto J_, auto I_) {
    constexpr int j = CV(J_), i = CV(I_);
    u32x4 rs = rs_w4;
    rs[2] = ib - 2 + j >= 0 ? (unsigned)WS_EP : 0u;
    dma_sc1<KVB + j * 16384 + i * 1024, (int)WS_KV + ((j - 2) * 256 + i) * FR>(L0 + w * 2048, ((ib * 8 + idx) * 32 + tyB * 16 + w * 2) * FR, vo_f, rs);
  };
  u32x4 qf;
  u32x2 xres[4];
  auto emit = [&](auto R_, auto P_) {
    static_assert(tab_of(CV(R_)).find(CV(P_), 2) < 0, "schedule");
    static_for<0, 2>([&](auto I_) {
      constexpr int ix = tab_of(CV(R_)).find(CV(P_), CV(I_));
      if constexpr (ix >= 0) {
        constexpr Ev e = tab_of(CV(R_)).e[ix];
        if constexpr (e.kind == K_STG) { if constexpr (CV(R_)) issueB(IC<e.a>{}, IC<e.b>{}); else issueA(IC<e.a>{}, IC<e.b>{}); }
        if constexpr (e.kind == K_W2) issueW(IC<e.a>{});
        if constexpr (e.kind == K_F) issueF(IC<e.a>{}, IC<e.b>{});
        if constexpr (e.kind == K_KV) issueKV(IC<e.a>{}, IC<e.b>{});
        if constexpr (e.kind == K_Q) ld_sc1<(int)WS_Q>(qf, ((ib * 8 + idx) * 8 + w) * FR, vo_f, rs_w4);
        if constexpr (e.kind == K_CS) dma_sc1<CSB, e.a * 8192>(lb1, cs_g + w * 1024, vo_l, rs_w4);
        if constexpr (e.kind == K_RS) dma_sc1_mask<RSB, (int)WS_RS>(0xffffu, lbq, ib * 2048 + w * 256, vo_l, rs_w4);
        if constexpr (e.kind == K_POLL) poll_issue(e.a == 0 ? 0x7f00u : e.a == 1 ? 3u : e.a == 2 ? 0xfcu : 0xfu);
        if constexpr (e.kind == K_TP) dma4<DUM, 24 * 128>((b * 2 + ((w >> 1) & 1)) * PF + n * 256, vo_l & 255, rs_p);
        if constexpr (e.kind == K_TW && e.a < 7) { unsigned i = e.a + xcd; i = i >= 7 ? i - 7 : i; i = i >= 7 ? i - 7 : i; dma4<DUM, 0>((i * 256 + rr * 8 + w) * PX, vo_l, mk_rs(wd, 1792 * PX)); }
        if constexpr (e.kind == K_TW && e.a >= 7) dma4<DUM, 0>((((e.a + xcd) & 1) * 256 + rr * 8 + w) * PX, vo_l, mk_rs(wo, 512 * PX));
      }
    });
  };

  if (tyB) {
    auto rdp = [&](auto G_, auto T_) {
      constexpr int g = CV(G_), t = CV(T_);
      if constexpr (g < 64) {
        constexpr int st_ = g < 32 ? g / 4 : 8 + (g - 32) / 8;
        constexpr int off = (slotB(st_) & 1) * S32 + (g < 32 ? (g & 3) : (g & 7)) * 2048;
        wf[g & 1][t] = tr_frag(L + ag[t] + off, 512);
        constexpr bool lastg = g < 32 ? (g & 7) == 7 : (g & 15) == 15;
        if constexpr (lastg && g < 63) { ag[t] += (slotB(st_) & 2) ? -65536 : 65536; pinu(ag[t]); }
      }
    };
    auto rdx = [&](auto G_) {
      constexpr int g = CV(G_);
      if constexpr (g < 32) {
        xf[g] = *(LAS bf16x8*)(L + ax[g & 3] + (slotB(g / 4) & 1) * S32);
        if constexpr ((g & 4) && g < 28) { ax[g & 3] += (slotB(g / 4) & 2) ? -65536 : 65536; pinu(ax[g & 3]); }
      }
    };
    unsigned ah;
    auto rdh = [&](auto G_) {
      constexpr int u = CV(G_) - 64;
      if constexpr (u < 32) wf[u & 1][(u >> 1) & 1] = tr_frag(L + ah + (u < 16 ? S32 : 0) + (u & 15) * 1024, 256);
    };
    set_ax(65536 + 8192 + (m & 1) * 4096 + (m >> 1) * 16384);
    set_ag(IC<0>{}, 65536); set_ag(IC<1>{}, 0);
    f32x16 accA[2] = {zero16, zero16}, accB[2] = {zero16, zero16}, accH = zero16;
    bf16x8 cc[2];
    stats(IC<4>{}, [&](auto K_) { emit(IC<1>{}, IC<-80 + CV(K_)>{}); });
    WB((TB.younger(K_STG, 0, 0)), 0);
    STAMP(1);
    rdx(IC<0>{}); rdp(IC<0>{}, IC<0>{}); rdp(IC<0>{}, IC<1>{});
    rdx(IC<1>{}); rdp(IC<1>{}, IC<0>{}); rdp(IC<1>{}, IC<1>{});
    auto fill = [&](auto Q_) {
      constexpr int q = CV(Q_);
      if constexpr (q == 0) jobp(IC<0>{}, IC<2>{}, ag[0] + S32, ag[1] + S32, 0, 2048, IC<0>{}, 0);
      if constexpr (q == 5) jobp(IC<1>{}, IC<2>{}, 0, 0, 0, 0, IC<0>{}, 0);
      if constexpr (q == 9) jobp(IC<2>{}, IC<2>{}, 0, 0, 0, 0, IC<0>{}, 0);
      if constexpr (q == 45) get_rs(IC<0>{});
      if constexpr (q >= 49 && q < 57 && (q & 1)) get_rs(IC<(q - 47) / 2>{});
      if constexpr (q == 57) { ah = ((vo_l & 704) | ((vo_l & 256) << 6)) + 65536; pinu(ah); }
      if constexpr (q == 59) { ah |= (((vo_l >> 3) & 32) ^ (32 * n)) | ((vo_l >> 1) & 24); pinu(ah); }
      if constexpr (q == 61) jobp(IC<0>{}, IC<2>{}, ag[0] + S32, ag[1] + S32, 0, 2048, IC<1>{}, 0);
      if constexpr (q == 63) { jobp(IC<1>{}, IC<2>{}, 0, 0, 0, 0, IC<1>{}, 0); get_cs(IC<0>{}, IC<0>{}, IC<2>{}); }
      if constexpr (q == 66) { get_cs(IC<1>{}, IC<0>{}, IC<2>{}); jobp(IC<2>{}, IC<2>{}, 0, 0, 0, 0, IC<1>{}, 0); }
      if constexpr (q == 67) { get_cs(IC<2>{}, IC<0>{}, IC<2>{}); corr2(IC<0>{}, csum[0], cc[0]); }
      if constexpr (q == 68) corr2(IC<1>{}, csum[0], cc[0]);
      if constexpr (q == 69) { accA[0] = MFMA(cc[0], mB, accA[0]); get_cs(IC<3>{}, IC<0>{}, IC<2>{}); corr2(IC<0>{}, csum[1], cc[1]); }
      if constexpr (q == 70) corr2(IC<1>{}, csum[1], cc[1]);
      if constexpr (q == 71) {
        accA[1] = MFMA(cc[1], mB, accA[1]);
        vo_p = (16 * (w & 3) + (vo_l >> 6)) * PF + ((vo_l & 48) ^ (32 * mat)); pinu(vo_p);
      }
      if constexpr (q >= 72 && q < 112) gelu20(IC<(q - 72) % 20>{}, accA[(q - 72) / 20], 4 * P0 + 2 * n + (q - 72) / 20);
      if constexpr (q == 109) jobp(IC<0>{}, IC<1>{}, ah + S32, 0, 0, 1024, IC<2>{}, 0);
      if constexpr (q == 112) { get_cs(IC<0>{}, IC<1>{}, IC<2>{}); jobp(IC<1>{}, IC<1>{}, 0, 0, 0, 0, IC<2>{}, 0); }
      if constexpr (q == 113) set_vw();
      if constexpr (q == 115) { get_cs(IC<1>{}, IC<1>{}, IC<2>{}); jobp(IC<2>{}, IC<1>{}, 0, 0, 0, 0, IC<2>{}, 0); }
      if constexpr (q == 116) get_cs(IC<2>{}, IC<1>{}, IC<2>{});
      if constexpr (q == 117) corr2(IC<0>{}, csum[0], cc[0]);
      if constexpr (q == 118) corr2(IC<1>{}, csum[0], cc[0]);
      if constexpr (q == 119) { accB[0] = MFMA(cc[0], mB, accB[0]); get_cs(IC<3>{}, IC<1>{}, IC<2>{}); }
      if constexpr (q == 120) corr2(IC<0>{}, csum[1], cc[1]);
      if constexpr (q == 121) corr2(IC<1>{}, csum[1], cc[1]);
      if constexpr (q == 122) accB[1] = MFMA(cc[1], mB, accB[1]);
      if constexpr (q >= 122 && q < 127) set_fw(IC<q - 122>{});
      if constexpr (q == 126) set_aw(IC<0>{});
      if constexpr (q == 127) { TS(43); wait_vm<TB.younger(K_EXT, X_H1, q)>(); TS(44); publish(1); }
    };
    static_for<0, 64>([&](auto G_) {
      constexpr int g = CV(G_);
      constexpr int st_ = g < 32 ? g / 4 : 8 + (g - 32) / 8;
      constexpr int pos = g < 32 ? g % 4 : (g - 32) % 8;
      constexpr bool bar = g < 32 ? (pos == 2) : (pos == 6);
      f32x16 (&acc)[2] = g < 32 ? accA : accB;
      if constexpr (bar) {
        constexpr int N0 = TB.younger(K_STG, st_ + 1, 2 * g);
        constexpr int N = st_ == 5 ? imin(N0, TB.younger(K_RS, 0, 2 * g)) : st_ == 7 ? imin(N0, TB.younger(K_CS, 0, 2 * g))
                        : st_ == 10 ? TB.younger(K_CS, 1, 2 * g) : N0;
        static_assert(N >= 0, "schedule");
        WB(N, st_ + 1);
        SBAR();
      }
      acc[0] = MFMA(wf[g & 1][0], xf[g & 31], acc[0]);
      SBAR();
      rdx(IC<g + 2>{}); rdp(IC<g + 2>{}, IC<0>{});
      if constexpr (g >= 62) rdh(IC<64 + (g & 1)>{});
      emit(IC<1>{}, IC<2 * g>{});
      fill(IC<2 * g>{});
      SBAR();
      acc[1] = MFMA(wf[g & 1][1], xf[g & 31], acc[1]);
      SBAR();
      rdp(IC<g + 2>{}, IC<1>{});
      if constexpr (g >= 62) rdh(IC<66 + (g & 1)>{});
      emit(IC<1>{}, IC<2 * g + 1>{});
      fill(IC<2 * g + 1>{});
      SBAR();
      if constexpr (g == 31) STAMP(2);
    });
    STAMP(3);
    static_for<0, 32>([&](auto U_) {
      constexpr int u = CV(U_);
      if constexpr (u == 12) { WB((TB.younger(K_STG, 13, 128 + u)), 13); SBAR(); }
      if constexpr (u == 28) { WB((TB.younger(K_CS, 2, 128 + u)), 14); SBAR(); }
      accH = MFMA(wf[u & 1][(u >> 1) & 1], xf[u], accH);
      SBAR();
      rdh(IC<68 + u>{});
      if constexpr (u == 23) { TS(4); wait_vm<TB.younger(K_POLL, 0, 128 + u)>(); TS(5); poll_read(); }
      if constexpr (u == 25) { poll_check(n ? 0u : kvm, n ? kvm : 0u); TS(6); }
      emit(IC<1>{}, IC<128 + u>{});
      if constexpr (u == 0) set_aw(IC<1>{});
      if constexpr (u == 24) vo_f = spread(vo_l);
      if constexpr (u == 28) get_cs(IC<0>{}, IC<2>{}, IC<1>{});
      if constexpr (u == 31) { get_cs(IC<1>{}, IC<2>{}, IC<1>{}); get_cs(IC<2>{}, IC<2>{}, IC<1>{}); corr2(IC<0>{}, csum[0], cc[0]); corr2(IC<1>{}, csum[0], cc[0]); }
      if constexpr (u >= 1) gelu20(IC<(u - 1) % 20>{}, accB[(u - 1) / 20], 4 * P0 + 4 + 2 * n + (u - 1) / 20);
      SBAR();
    });
    STAMP(4); TS(7);
    __builtin_amdgcn_s_setprio(2);
    accH = MFMA(cc[0], mB, accH);
    SBAR();
    static_for<0, 4>([&](auto J_) { rdW(J_, IC<0>{}); rdW(J_, IC<1>{}); });
    SBAR();
    static_for<0, 25>([&](auto I_) {
      constexpr int i = CV(I_);
      if constexpr (i == 12) { land1<TB.younger(K_Q, 0, PT(i))>(qf); WB(63, 15); }
      emit(IC<1>{}, IC<PT(i)>{});
      if constexpr (i < 9) gelu20(IC<11 + i>{}, accB[1], 4 * P0 + 4 + 2 * n + 1);
      else gelu16(IC<i - 9>{}, accH, 96 + 2 * idx + n);
    });
    STAMP(5);
  } else {
    unsigned aq[2];
    {
      const int lz = lane_again();
      const int h = lz >> 5, blk = (lz >> 4) & 1, tq = (lz & 15) >> 2, tp = lz & 3;
      aq[0] = (8 * h + tq) * 128 + (((4 * n + 2 * blk + (tp >> 1)) ^ (4 * ((tq >> 1) & 1))) * 16) + (tp & 1) * 8;
      aq[1] = aq[0] + 65536;
      pinu(aq[0]); pinu(aq[1]);
    }
    auto rdp = [&](auto G_, auto T_) {
      constexpr int g = CV(G_), t = CV(T_);
      if constexpr (g < 32) {
        wf[g & 1][t] = tr_frag(L + ag[t] + ((g >= 8 && g < 24) ? S32 : 0) + (g & 7) * 2048, 512);
        if constexpr (g == 15) { ag[t] += 65536; pinu(ag[t]); }
      }
    };
    auto rdq = [&](auto G_, auto T_) {
      constexpr int g = CV(G_), t = CV(T_);
      if constexpr (g < 32) {
        constexpr int rg = (g / 4 + 1) % 3, off = rg * RING + (rg == 0 ? 0 : 16384) + t * 8192 + (g & 3) * 2048;
        wf[g & 1][t] = tr_frag(L + aq[off >> 16] + (off & 65535), 512);
      } else if constexpr (t < 2) rdp(IC<g - 32>{}, T_);
    };
    auto rdx = [&](auto G_) {
      constexpr int g = CV(G_), rg = (g / 4 + 1) % 3;
      if constexpr (g < 32) xf[g] = *(LAS bf16x8*)(L + ax[g & 3] + (rg == 0 ? 0 : rg * RING - 24576));
    };
    set_ax(24576 + m * 4096);
    set_vo(lane_again());
    f32x16 accQ[3] = {zero16, zero16, zero16}, acc[2] = {zero16, zero16};
    bf16x8 cc[2];
    float rq;
    pinu(vo_x); pinu(vo_p); pinu(vo_q);
    stats(IC<5>{}, [&](auto K_) { emit(IC<0>{}, IC<-80 + CV(K_)>{}); });
    WB((TA.younger(K_STG, 0, 0)), 0);
    STAMP(1);
    rdx(IC<0>{}); rdq(IC<0>{}, IC<0>{}); rdq(IC<0>{}, IC<1>{}); rdq(IC<0>{}, IC<2>{});
    rdx(IC<1>{}); rdq(IC<1>{}, IC<0>{}); rdq(IC<1>{}, IC<1>{}); rdq(IC<1>{}, IC<2>{});
    auto fillq = [&](auto Q_) {
      constexpr int q = CV(Q_);
      if constexpr (q == 0) jobp(IC<0>{}, IC<3>{}, aq[0] + RING + 16384, aq[1], aq[1] + 8192, 2048, IC<0>{}, 0);
      if constexpr (q == 7) jobp(IC<1>{}, IC<3>{}, 0, 0, 0, 0, IC<0>{}, 0);
      if constexpr (q == 11) jobp(IC<2>{}, IC<3>{}, 0, 0, 0, 0, IC<0>{}, 0);
      if constexpr (q == 62 || q == 63) set_ag(IC<q - 62>{}, 0);
      if constexpr (q == 67) get_rs(IC<0>{});
      if constexpr (q >= 71 && q < 75) get_rs(IC<q - 70>{});
      if constexpr (q == 77) { rq = rstd * (0.25f * 1.4426950408889634f); pinf(rq); }
      if constexpr (q == 79) get_cs(IC<0>{}, IC<0>{}, IC<3>{});
      if constexpr (q == 83) get_cs(IC<1>{}, IC<0>{}, IC<3>{});
      if constexpr (q == 84) get_cs(IC<2>{}, IC<0>{}, IC<3>{});
      if constexpr (q == 85) corr2(IC<0>{}, csum[0], cc[0]);
      if constexpr (q == 86) corr2(IC<1>{}, csum[0], cc[0]);
      if constexpr (q == 87) get_cs(IC<3>{}, IC<0>{}, IC<3>{});
      if constexpr (q == 88) { accQ[0] = MFMA(cc[0], mB, accQ[0]); corr2(IC<0>{}, csum[1], cc[1]); }
      if constexpr (q == 89) corr2(IC<1>{}, csum[1], cc[1]);
      if constexpr (q == 90) get_cs(IC<4>{}, IC<0>{}, IC<3>{});
      if constexpr (q == 91) corr2(IC<0>{}, csum[2], cc[0]);
      if constexpr (q == 92) { accQ[1] = MFMA(cc[1], mB, accQ[1]); corr2(IC<1>{}, csum[2], cc[0]); }
      if constexpr (q == 93) accQ[2] = MFMA(cc[0], mB, accQ[2]);
    };
    static_for<0, 32>([&](auto G_) {
      constexpr int g = CV(G_), st_ = g / 4, pos = g % 4;
      if constexpr (pos == 2) {
        constexpr int N0 = TA.younger(K_STG, st_ + 1, 3 * g);
        constexpr int N = st_ == 5 ? imin(N0, TA.younger(K_RS, 0, 3 * g)) : st_ == 6 ? imin(N0, TA.younger(K_CS, 0, 3 * g)) : N0;
        static_assert(N >= 0, "schedule");
        WB(N, st_ + 1);
        SBAR();
      }
      static_for<0, 3>([&](auto K_) {
        constexpr int k = CV(K_);
        accQ[k] = MFMA(wf[g & 1][k], xf[g], accQ[k]);
        SBAR();
        if constexpr (k == 0) rdx(IC<g + 2>{});
        rdq(IC<g + 2>{}, K_);
        emit(IC<0>{}, IC<3 * g + k>{});
        fillq(IC<3 * g + k>{});
        SBAR();
      });
    });
    STAMP(2);
    const unsigned kvg = (unsigned)WS_KV + (ib * 256 + w * 2) * FR;
    auto qkv_slice = [&](auto I_) {
      constexpr int i = CV(I_), f = i >> 2, t = f < 4 ? 1 + (f & 1) : 0, X = f < 4 ? f >> 1 : 5 - f, o = 8 * X + 2 * (i & 3);
      hq[i & 3] = scale2(accQ[t][o], accQ[t][o + 1], t ? rstd : rq);
      if constexpr ((i & 3) == 3) {
        const unsigned pos = t == 2 ? vo_l ^ ((vo_l >> 9) * 64) : vo_l;
        if constexpr (f < 4) store_fr(rs_ws, kvg + qcol(t) * (FR / 4) + (X * 16 + t - 1) * FR, pos, hq);
        if constexpr (f == 4) store_fr(rs_ws, (unsigned)WS_Q + ((ib * 8 + idx) * 8 + w) * FR, pos, hq);
        if constexpr (f == 5) qf = hq;
      }
    };
    auto fillp = [&](auto Q_) {
      constexpr int q = CV(Q_);
      if constexpr (q < 24) qkv_slice(Q_);
      if constexpr (q == 16) jobp(IC<0>{}, IC<2>{}, ag[0] + S32, ag[1] + S32, 0, 2048, IC<1>{}, 0);
      if constexpr (q == 20) jobp(IC<1>{}, IC<2>{}, 0, 0, 0, 0, IC<1>{}, 0);
      if constexpr (q == 24) jobp(IC<2>{}, IC<2>{}, 0, 0, 0, 0, IC<1>{}, 2);
      if constexpr (q == 25) set_vw();
      if constexpr (q == 33) { TS(43); wait_vm<TA.younger(K_EXT, X_QKV, 96 + q)>(); TS(44); publish(0); }
      if constexpr (q >= 35 && q < 45 && (q & 1)) set_fw(IC<(q - 35) / 2>{});
      if constexpr (q == 45 || q == 47) set_aw(IC<(q - 45) / 2>{});
      if constexpr (q == 57) { TS(4); wait_vm<TA.younger(K_POLL, 0, 96 + q)>(); TS(5); poll_read(); }
      if constexpr (q == 58) vo_f = spread(vo_l);
      if constexpr (q == 59) { poll_check(n ? 0u : kvm, n ? kvm : 0u); TS(6); }
      if constexpr (q == 60) get_cs(IC<0>{}, IC<1>{}, IC<2>{});
      if constexpr (q == 62) { get_cs(IC<1>{}, IC<1>{}, IC<2>{}); get_cs(IC<2>{}, IC<1>{}, IC<2>{}); corr2(IC<0>{}, csum[0], cc[0]); corr2(IC<1>{}, csum[0], cc[0]); }
      if constexpr (q == 63) {
        acc[0] = MFMA(cc[0], mB, acc[0]);
        get_cs(IC<3>{}, IC<1>{}, IC<2>{}); corr2(IC<0>{}, csum[1], cc[1]); corr2(IC<1>{}, csum[1], cc[1]);
      }
    };
    static_for<0, 32>([&](auto G_) {
      constexpr int g = CV(G_);
      if constexpr ((g & 7) == 6) {
        WB((g == 30 ? TA.younger(K_CS, 1, PA(g, 0)) : TA.younger(K_STG, 9 + g / 8, PA(g, 0))), (g == 30 ? 14 : g == 22 ? 13 : g == 14 ? 12 : 9));
        SBAR();
      }
      static_for<0, 2>([&](auto K_) {
        constexpr int k = CV(K_);
        acc[k] = MFMA(wf[g & 1][k], xf[g], acc[k]);
        SBAR();
        rdp(IC<g + 2>{}, K_);
        emit(IC<0>{}, IC<PA(g, k)>{});
        fillp(IC<2 * g + k>{});
        SBAR();
      });
    });
    STAMP(4); TS(7);
    __builtin_amdgcn_s_setprio(2);
    acc[1] = MFMA(cc[1], mB, acc[1]);
    SBAR();
    static_for<0, 4>([&](auto J_) { rdW(J_, IC<0>{}); rdW(J_, IC<1>{}); });
    SBAR();
    static_for<0, 32>([&](auto I_) {
      constexpr int i = CV(I_);
      if constexpr (i == 16) WB((TA.younger(K_KV, 1, PT(i))), 15);
      emit(IC<0>{}, IC<PT(i)>{});
      gelu16(IC<i % 16>{}, acc[i / 16], 4 * idx + 2 * n + i / 16);
    });
    STAMP(5);
  }
  {
    unsigned ka = KVB + w * 2048 + vo_l, blk = (vo_l & 256) ? ~0u : 0u;
    unsigned va = KVB + w * 2048 + 1024 + ((vo_l & 16) << 5) + ((((vo_l >> 3) ^ (vo_l << 2)) & 64) | ((vo_l >> 2) & 56));
    va = blk ? 0u : va;
    pinu(ka); pinu(blk); pinu(va);
    TS(8);
    if (YNG) {
      __builtin_amdgcn_s_setprio(0);
      static_for<0, 20>([&](auto Q_) {
        constexpr int q = CV(Q_);
        if constexpr (q == 4) { TS(45); wait_vm<5>(); TS(46); poll_read(); }
        if constexpr (q == 6) { TS(47); poll_check(3u, 3u); TS(48); }
        if constexpr (q == 10) { TS(49); wait_vm<10>(); TS(50); publish(3); if (tyB) publish(2); }
        emit(IC<2>{}, Q_);
      });
      TS(51);
      __builtin_amdgcn_s_setprio(2);
    }
    STAMP(6);
    const bf16x8 q = __builtin_bit_cast(bf16x8, qf);
    const int kt0 = 8 - (ib * 4 + m);
    auto kfr = [&](int t) { return *(LAS bf16x8*)(L + ka + t * 4096); };
    auto vaddr = [&](int t) {
      unsigned a = (blk & (unsigned)((t >= kt0 ? ONES : KVB) - t * 4096)) | va;
      pinu(a);
      return a + t * 4096;
    };
    f32x16 o = zero16;
    {
      bf16x8 kf[2], vf[2][2];
      f32x16 sb[2], s8;
      kf[1] = kfr(1); kf[0] = kfr(2);
      { const unsigned a = vaddr(1); vf[1][0] = tr_frag(L + a, 128); vf[1][1] = tr_frag(L + a + 256, 128); }
      sb[1] = MFMA(kf[1], q, zero16);
      SBAR();
      kf[1] = kfr(3);
      asm volatile("s_nop 7" ::: "memory");
      SBAR();
      auto vm_op = [&](auto Q_) {
        constexpr int q = CV(Q_);
        if constexpr (q == 20) { if (!YNG) { TS(49); wait_vm<0>(); TS(50); publish(3); if (tyB) publish(2); } }
        if constexpr (q == 24) { if (YNG) poll_issue(0xfcu); }
      };
      static_for<1, 8>([&](auto T_) {
        constexpr int t = CV(T_), tn = t + 1 < 8 ? t + 1 : 0;
        sb[(t + 1) & 1] = MFMA(kf[(t + 1) & 1], q, zero16);
        SBAR();
        if constexpr (t + 3 <= 9) kf[(t + 1) & 1] = kfr(t + 3 < 8 ? t + 3 : t + 3 == 8 ? 0 : 8);
        { const unsigned a = vaddr(tn); vf[(t + 1) & 1][0] = tr_frag(L + a, 128); vf[(t + 1) & 1][1] = tr_frag(L + a + 256, 128); }
        if constexpr (t == 1 || t == 4) TS(10 + 2 * t);
        u32x4 p0, p1;
        const f32x16& s = sb[t & 1];
        unsigned r0, r1;
        exp_pk2(r0, r1, s[0], s[1], s[2], s[3]); p0[0] = r0; p0[1] = r1;
        vm_op(IC<4 * t>{});
        exp_pk2(r0, r1, s[4], s[5], s[6], s[7]); p0[2] = r0; p0[3] = r1;
        vm_op(IC<4 * t + 1>{});
        SBAR();
        o = MFMA(vf[t & 1][0], __builtin_bit_cast(bf16x8, p0), o);
        SBAR();
        exp_pk2(r0, r1, s[8], s[9], s[10], s[11]); p1[0] = r0; p1[1] = r1;
        vm_op(IC<4 * t + 2>{});
        exp_pk2(r0, r1, s[12], s[13], s[14], s[15]); p1[2] = r0; p1[3] = r1;
        vm_op(IC<4 * t + 3>{});
        SBAR();
        if constexpr (t == 7) s8 = MFMA(kf[1], q, zero16);
        o = MFMA(vf[t & 1][1], __builtin_bit_cast(bf16x8, p1), o);
        SBAR();
      });
      { const unsigned a = vaddr(8); vf[1][0] = tr_frag(L + a, 128); vf[1][1] = tr_frag(L + a + 256, 128); }
      asm volatile("s_nop 7" ::: "memory");
      TS(26);
      static_for<0, 2>([&](auto H_) {
        constexpr int hh = CV(H_);
        u32x4 pd, pf;
        static_for<0, 4>([&](auto J_) {
          constexpr int r = 8 * hh + 2 * CV(J_);
          unsigned d, f;
          exp_tri(d, f, s8[r], s8[r + 1], sb[0][r], sb[0][r + 1], tri_mask(r), tri_mask(r + 1));
          pd[CV(J_)] = d; pf[CV(J_)] = f;
          vm_op(IC<32 + 4 * hh + CV(J_)>{});
        });
        SBAR();
        o = MFMA(vf[0][hh], __builtin_bit_cast(bf16x8, pf), o);
        o = MFMA(vf[1][hh], __builtin_bit_cast(bf16x8, pd), o);
        SBAR();
      });
    }
    TS(27);
    float amax = __builtin_fabsf(o[0]);
#pragma unroll
    for (int r = 1; r < 8; ++r) amax = __builtin_fmaxf(amax, __builtin_fabsf(o[r]));
    if (RARE(~BALLOT((o[8] >= 0x1p-64f) & (o[8] <= 0x1p64f) & (amax <= 0x1p126f)))) {
      const int qq = ((vo_l >> 4) & 31) - 4 * (vo_l >> 9);
      auto score = [&](int t, const f32x16& cin) {
        f32x16 s = MFMA(kfr(t), q, cin);
#pragma unroll
        for (int r = 0; r < 16; ++r) {
          const bool le = ((r & 3) + 8 * (r >> 2)) <= qq;
          s[r] = ((t == 8 && !le) || (t == 0 && le)) ? -INFINITY : s[r];
        }
        return s;
      };
      float mx = -INFINITY;
#pragma unroll 1
      for (int t = kt0 < 0 ? 0 : kt0; t < 9; ++t) {
        const f32x16 s = score(t, zero16);
#pragma unroll
        for (int r = 0; r < 16; ++r) mx = __builtin_fmaxf(mx, s[r]);
      }
      mx = -__builtin_fmaxf(mx, swap32(mx));
      f32x16 nm;
#pragma unroll
      for (int r = 0; r < 16; ++r) nm[r] = mx;
      o = zero16;
#pragma unroll 1
      for (int t = kt0 < 0 ? 0 : kt0; t < 9; ++t) {
        const f32x16 s = score(t, nm);
        u32x4 p0, p1;
#pragma unroll
        for (int j = 0; j < 4; ++j) {
          p0[j] = pk2(__builtin_amdgcn_exp2f(s[2 * j]), __builtin_amdgcn_exp2f(s[2 * j + 1]));
          p1[j] = pk2(__builtin_amdgcn_exp2f(s[8 + 2 * j]), __builtin_amdgcn_exp2f(s[9 + 2 * j]));
        }
        const unsigned a = vaddr(t);
        o = MFMA(tr_frag(L + a, 128), __builtin_bit_cast(bf16x8, p0), o);
        o = MFMA(tr_frag(L + a + 256, 128), __builtin_bit_cast(bf16x8, p1), o);
      }
    }
    const float rl = __builtin_amdgcn_rcpf(o[8]);
    const u32x4 of = {pk2(o[0] * rl, o[1] * rl), pk2(o[2] * rl, o[3] * rl), pk2(o[4] * rl, o[5] * rl), pk2(o[6] * rl, o[7] * rl)};
    store_sc1(rs_ws, vo_f + a2row + (112 + idx * 4 + 2 * n + tyB) * FR, of);
    __hip_atomic_fetch_add((LAS unsigned*)(L + CNT), 1u, __ATOMIC_RELAXED, __HIP_MEMORY_SCOPE_WORKGROUP);
  }
  STAMP(7);

  {
    TS(28);
    __builtin_amdgcn_s_setprio(1);
    if (!YNG) {
      poll_read();
      static_for<0, 20>([&](auto Q_) {
        constexpr int q = CV(Q_);
        if constexpr (q == 6) poll_check(3u, 3u);
        if constexpr (q == 10) { wait_vm<10>(); publish(4); __builtin_amdgcn_s_setprio(0); poll_issue(0xfcu); }
        emit(IC<2>{}, Q_);
      });
      wait_vm<10>();
    } else wait_vm<1>();
    TS(29);
    poll_read();
    unsigned xa[2], q0[2], done;
    auto put = [&](auto K_, auto G_) {
      constexpr int k = CV(K_), g = CV(G_);
      const f32x16& a = acc2[k >> 1][k & 1];
      *(LAS f32x4*)(L + (xa[k & 1] ^ (g * 32)) + (k == 0 || k == 3 ? S32 : 0)) = (f32x4){a[4 * g], a[4 * g + 1], a[4 * g + 2], a[4 * g + 3]};
    };
    auto mark = [&](int ph) {
      asm volatile("" ::: "memory");
      *(volatile LAS unsigned*)(L + CNT + 32 + ph * 32 + w * 4) = 1u;
    };
    auto await = [&](int off) {
#pragma unroll 1
      for (unsigned spin = 0; spin < (1u << 20); ++spin) {
        const u32x4 f = *(volatile LAS u32x4*)(L + CNT + off);
        if (__builtin_amdgcn_readfirstlane(f[0] & f[1] & f[2] & f[3])) break;
      }
      asm volatile("" ::: "memory");
    };
    static_for<0, 72>([&](auto I_) {
      constexpr int i = CV(I_);
      constexpr int j = i < 56 ? i >> 2 : 14 + (i & 3), k = i < 56 ? i & 3 : ((i >> 2) & 3);
      if constexpr (i == 16) TS(33);
      if constexpr (i == 28) TS(34);
      if constexpr (i == 48) TS(57);
      if constexpr (i == 56) TS(37);
      if constexpr ((i & 3) == 0 && i < 48) land2<ycom(K_F, j, 48 + i)>(af[j][0], af[j][1]);
      if constexpr (i == 48) {
        if (YNG) wait_vm<TY.younger(K_W2, 35, 48 + i)>();
        land2<TO.younger(K_W2, 35, 48 + i)>(af[12][0], af[12][1]); land2<63>(af[13][0], af[13][1]);
      }
      if constexpr (i >= 56 && !(k & 1)) land1<(k ? 7 : 3) - (i & 3)>(af[j][k >> 1]);
      mm2(IC<j>{}, IC<k>{});
      SBAR();
      if constexpr (i == 8) wait_vm<ycom(K_W2, 19, 48 + i)>();
      if constexpr (i < 6) rdW(IC<4 + i / 2>{}, IC<i & 1>{});
      if constexpr (i >= 8 && i < 14) rdW(IC<3 + i / 2>{}, IC<i & 1>{});
      if constexpr (i == 1) { poll_check(0xcu, 0xcu); TS(30); }
      if constexpr (i == 7) { TS(31); if (YNG) { wait_vm<6>(); publish(4); __builtin_amdgcn_s_setprio(0); } TS(32); }
      if constexpr (i == 11) poll_check(0xf0u, 0xf0u);
      if constexpr (i == 28) { if (YNG) __builtin_amdgcn_s_setprio(1); }
      if constexpr (i == 12) {
        fwo = (vo_l & 16) * 256 + ((vo_l >> 1) & 16) + (((p >> 1) * 16 + kw) * 64 + 32 + 4 * (p & 1)) * 8; pinu(fwo);
      }
      if constexpr (i == 10 || i == 22 || i == 36) wait_lds();
      if constexpr (i == 30) wait_vm<ycom(K_W2, 23, 48 + i)>();
      if constexpr (i >= 30 && i < 34) rdW(IC<10 + (i - 30) / 2>{}, IC<i & 1>{});
      if constexpr (i == 38) wait_vm<ycom(K_W2, 27, 48 + i)>();
      if constexpr (i >= 38 && i < 42) rdW(IC<12 + (i - 38) / 2>{}, IC<i & 1>{});
      if constexpr (i == PY + 11) { if (YNG) { TS(35); wait_vm<TY.younger(K_POLL, 4, 48 + i)>(); TS(36); } poll_read(); }
      if constexpr (i == 42) { if (!YNG) { TS(35); wait_vm<TO.younger(K_POLL, 4, 48 + i)>(); TS(36); } poll_read(); }
      if constexpr (i == PY + 13) poll_check(mat * 15u, mat * 15u);
      if constexpr (i == 44) { STAMP(8); poll_check(15u - mat * 15u, 15u - mat * 15u); STAMP(9); TS(38); }
      emit(IC<2>{}, IC<48 + i>{});
      if constexpr (i == PY) { if (YNG) poll_issue(0xfu); }
      if constexpr (i == 35) { if (!YNG) poll_issue(0xfu); }
      if constexpr (i == 30 || i == 40) {
        if (!YNG) static_for<0, 2>([&](auto Z_) { ld8<((i - 30) / 5 + CV(Z_)) * 16 * PX>(xres[(i - 30) / 5 + CV(Z_)], p * 64 * PX + c * 128, (tid >> 4) * PX + (tid & 15) * 8, rs_x); });
      }
      if constexpr (fy_at(48 + i) >= 0) { if (YNG) issueF(IC<14 + (fy_at(48 + i) & 3)>{}, IC<1 - fy_at(48 + i) / 4>{}); }
      if constexpr (i >= 44 && i < 52) { if (!YNG) issueF(IC<14 + (i & 3)>{}, IC<1 - (i - 44) / 4>{}); }
      if constexpr (i >= 48 && i < 56) rdW(IC<14 + (i & 3)>{}, IC<(i - 48) / 4>{});
      if constexpr (i == 45) { xa[0] = w * 4096 + (vo_l & 496) * 8 + (((vo_l >> 5) ^ vo_l) & 112); xa[1] = xa[0] + 65536; pinu(xa[0]); pinu(xa[1]); }
      if constexpr (i == 46) { q0[1] = (tid >> 4) * 128 + (((tid ^ (tid >> 4)) & 7) * 16); pinu(q0[1]); }
      if constexpr (i == 47) { q0[0] = q0[1] + (tid & 8) * (S32 / 8) + S32; q0[1] += (tid & 8) * (3 * S32 / 8); pinu(q0[0]); pinu(q0[1]); }
      if constexpr (i == 57) done = *(volatile LAS unsigned*)(L + CNT);
      if constexpr (i == 64) {
        if (RARE(__builtin_amdgcn_readfirstlane(done) != 512)) {
#pragma unroll 1
          for (unsigned spin = 0; spin < (1u << 20); ++spin)
            if (__builtin_amdgcn_readfirstlane(*(volatile LAS unsigned*)(L + CNT)) == 512) break;
        }
      }
      if constexpr (i >= 60 && (i & 3) >= 2) { put(IC<(k + 3) & 3>{}, IC<2 * (i & 1)>{}); put(IC<(k + 3) & 3>{}, IC<2 * (i & 1) + 1>{}); }
      if constexpr (i == 67) mark(0);
      SBAR();
    });
    STAMP(10); TS(41);
    static_for<0, 4>([&](auto G_) { put(IC<1>{}, G_); });
    mark(1);
    TS(53);
    if (!YNG) {
      u32x4 seen = {tlo, thi, tlo, thi};
      if (b == 0) seen = load_sc1(rs_ws, (unsigned)WS_FL + tid * 512 + 192);
      landed(xres[0], xres[1]); landed(xres[2], xres[3]);
      auto sum4q = [&](unsigned a) {
        return (*(LAS f32x4*)(L + a) + *(LAS f32x4*)(L + a + 4096)) + (*(LAS f32x4*)(L + a + 8192) + *(LAS f32x4*)(L + a + 12288));
      };
      f32x4 pre[4];
      await(64);
#pragma unroll
      for (int z = 0; z < 4; ++z) {
        pre[z] = sum4q(q0[z >> 1] + (z & 1) * 2048) + (f32x4){__uint_as_float(xres[z][0] << 16), __uint_as_float(xres[z][0] & 0xffff0000u),
                                                           __uint_as_float(xres[z][1] << 16), __uint_as_float(xres[z][1] & 0xffff0000u)};
        pinq(pre[z]);
      }
      unsigned oo = (p * 64 + (tid >> 4)) * PX + c * 128 + (tid & 15) * 8;
      pinu(oo);
      TS(54);
      const __amdgpu_buffer_rsrc_t rs_o = __builtin_amdgcn_make_buffer_rsrc(out, 0, 2097152, 0x00020000);
#pragma unroll
      for (int ph = 0; ph < 2; ++ph) {
        await(48 + ph * 32);
        if (ph) TS(55);
#pragma unroll
        for (int z = 2 - 2 * ph; z < 4 - 2 * ph; ++z) {
          const f32x4 v = sum4q(q0[z >> 1] + (z & 1) * 2048 + 16384) + pre[z];
          __builtin_amdgcn_raw_buffer_store_b64((u32x2){pk2(v[0], v[1]), pk2(v[2], v[3])}, rs_o, oo + z * 16 * PX, 0, 16);
        }
      }
      TS(42);
      if (b == 0) {
#pragma unroll 1
        for (unsigned spin = 0; spin < (1u << 17) && !__all((seen[0] == tlo) & (seen[1] == thi)); ++spin) {
          __builtin_amdgcn_s_sleep(1);
          seen = load_sc1(rs_ws, (unsigned)WS_FL + tid * 512 + 192);
        }
        *(volatile LAS unsigned*)(L + CNT + 96 + w * 4) = 1u;
        if (w == 0) {
          await(96);
          if (tid == 0) __hip_atomic_store((GAS u64*)(ws + WS_EP), (u64)thi << 32 | tlo, RLX_AGENT);
        }
      }
    }
  }
  asm volatile(".Lk_end:");
  STAMP(11);
  STAMP_OUT(0);
}

extern "C" void kernel_launch(void* const* d_in, const int* in_sizes, int n_in, void* d_out, int out_size,
                              void* d_ws, size_t ws_size, hipStream_t stream) {
  fused<<<256, 512, 0, stream>>>((const char*)d_in[0], (const char*)d_in[1], (const char*)d_in[2], (const char*)d_in[4],
                                 (const char*)d_in[5], (const char*)d_in[6], (char*)d_ws, (char*)d_out);
}
```
